```python
import torch

def _softmax(x, axis=-1):
    return torch.softmax(x, dim=axis)
def _gelu(x):
    return torch.nn.functional.gelu(x, approximate="tanh")
def _silu(x):
    return torch.nn.functional.silu(x)
def _sigmoid(x):
    return torch.sigmoid(x)
def _layernorm(x, axis=-1, eps=1e-5):
    m = x.mean(dim=axis, keepdim=True)
    v = x.var(dim=axis, keepdim=True, unbiased=False)
    return (x - m) / torch.sqrt(v + eps)
def _scatter_add(tgt, idx, src, axis=0):
    return tgt.clone().index_add_(axis, idx, src)
def _segment_sum(src, seg, n):
    out = torch.zeros((n,) + src.shape[1:], dtype=src.dtype)
    return out.index_add_(0, seg, src)
def _pad(x, pw):
    flat = tuple(v for pair in reversed(pw) for v in pair)
    return torch.nn.functional.pad(x, flat)

kernel_name = 'standalone__spectral_fnet__prompt'

def setup_inputs():

    _w = (lambda a: (lambda t: t.to(torch.bfloat16) if t.is_floating_point() else t)(torch.as_tensor(a)))
    B, S, D = 1, 2048, 1024
    ns = torch.arange(S)[:, None] * torch.arange(S)[None, :]
    nd = torch.arange(D)[:, None] * torch.arange(D)[None, :]
    Ws_re = torch.cos(-2.0 * torch.pi * ns / S).to(float)
    Ws_im = torch.sin(-2.0 * torch.pi * ns / S).to(float)
    Wd_re = torch.cos(-2.0 * torch.pi * nd / D).to(float)
    Wd_im = torch.sin(-2.0 * torch.pi * nd / D).to(float)
    return {
        "x": _w((lambda *s: torch.randn(*s, dtype=torch.bfloat16) * 0.05)(B, S, D)),
        "dft_seq_re": _w(Ws_re),
        "dft_seq_im": _w(Ws_im),
        "dft_hid_re": _w(Wd_re),
        "dft_hid_im": _w(Wd_im),
    }

def reference(x, dft_seq_re, dft_seq_im, dft_hid_re, dft_hid_im):
    a = torch.einsum("bsd,ed->bse", x, dft_hid_re)
    b = torch.einsum("bsd,ed->bse", x, dft_hid_im)
    y = torch.einsum("ts,bse->bte", dft_seq_re, a) - torch.einsum("ts,bse->bte", dft_seq_im, b)
    return y
```

```cpp
#include <hip/hip_runtime.h>
#include <hip/hip_bf16.h>

typedef unsigned short bf16_t;
typedef short bf16x8 __attribute__((ext_vector_type(8)));
typedef float f32x4 __attribute__((ext_vector_type(4)));
typedef unsigned u32x2 __attribute__((ext_vector_type(2)));

constexpr int S = 2048, D = 1024;

__device__ __forceinline__ unsigned cvt_pk_bf16(float lo, float hi) {
    typedef __bf16 bf16x2_t __attribute__((ext_vector_type(2)));
    typedef float f32x2_t __attribute__((ext_vector_type(2)));
    bf16x2_t r = __builtin_convertvector((f32x2_t){lo, hi}, bf16x2_t);
    return __builtin_bit_cast(unsigned, r);
}

template <int BQ>
__device__ __forceinline__ void gemm_tile(const bf16_t* __restrict__ P0, const bf16_t* __restrict__ P1, int ldp,
                                          const bf16_t* __restrict__ Q0, const bf16_t* __restrict__ Q1, int ldq,
                                          int khalf, int ktot, bf16_t* __restrict__ out, int ldo, float sign,
                                          unsigned char* shm) {
    const int tid = threadIdx.x, wid = tid >> 6, lane = tid & 63, wr = wid >> 1, wc = wid & 1, fr = lane & 15, fq = lane >> 4;
    constexpr int BK = 64;
    constexpr int PB = 128 * BK * 2, QB = BQ * BK * 2;
    constexpr int NQ = BQ / 32;
    f32x4 acc[4][NQ];
#pragma unroll
    for (int i = 0; i < 4; ++i)
#pragma unroll
        for (int j = 0; j < NQ; ++j) acc[i][j] = (f32x4){0.f, 0.f, 0.f, 0.f};
    auto stage = [&](int buf, int kt) {
        const int k0 = kt * BK;
        const bf16_t* Pb = k0 < khalf ? P0 + k0 : P1 + (k0 - khalf);
        const bf16_t* Qb = k0 < khalf ? Q0 + k0 : Q1 + (k0 - khalf);
        unsigned char* sp = shm + buf * (PB + QB);
        unsigned char* sq = sp + PB;
#pragma unroll
        for (int i = 0; i < PB / 4096; ++i) {
            const int b = tid * 16 + i * 4096, r = b >> 7, c = (b >> 4) & 7, cs = c ^ (r & 7);
            __builtin_amdgcn_global_load_lds((const unsigned*)(Pb + (size_t)r * ldp + cs * 8), (unsigned*)(sp + b), 16, 0, 0);
        }
#pragma unroll
        for (int i = 0; i < QB / 4096; ++i) {
            const int b = tid * 16 + i * 4096, r = b >> 7, c = (b >> 4) & 7, cs = c ^ (r & 7);
            __builtin_amdgcn_global_load_lds((const unsigned*)(Qb + (size_t)r * ldq + cs * 8), (unsigned*)(sq + b), 16, 0, 0);
        }
    };
    const int nt = ktot / BK;
    stage(0, 0);
    asm volatile("s_waitcnt vmcnt(0)" ::: "memory");
    __syncthreads();
    for (int t = 0; t < nt; ++t) {
        const int cur = t & 1;
        if (t + 1 < nt) stage(cur ^ 1, t + 1);
        const unsigned char* sp = shm + cur * (PB + QB);
        const unsigned char* sq = sp + PB;
#pragma unroll
        for (int kk = 0; kk < 2; ++kk) {
            bf16x8 a[4], b[NQ];
#pragma unroll
            for (int i = 0; i < 4; ++i) {
                const int r = wr * 64 + i * 16 + fr, c = (kk * 4 + fq) ^ (r & 7);
                a[i] = *(const bf16x8*)(sp + r * 128 + c * 16);
            }
#pragma unroll
            for (int j = 0; j < NQ; ++j) {
                const int r = wc * (BQ / 2) + j * 16 + fr, c = (kk * 4 + fq) ^ (r & 7);
                b[j] = *(const bf16x8*)(sq + r * 128 + c * 16);
            }
#pragma unroll
            for (int i = 0; i < 4; ++i)
#pragma unroll
                for (int j = 0; j < NQ; ++j)
                    acc[i][j] = __builtin_amdgcn_mfma_f32_16x16x32_bf16(a[i], b[j], acc[i][j], 0, 0, 0);
        }
        asm volatile("s_waitcnt vmcnt(0)" ::: "memory");
        __syncthreads();
    }
#pragma unroll
    for (int i = 0; i < 4; ++i)
#pragma unroll
        for (int j = 0; j < NQ; ++j) {
            const int p = wr * 64 + i * 16 + fq * 4, q = wc * (BQ / 2) + j * 16 + fr;
            u32x2 w;
            w.x = cvt_pk_bf16(acc[i][j][0] * sign, acc[i][j][1] * sign);
            w.y = cvt_pk_bf16(acc[i][j][2] * sign, acc[i][j][3] * sign);
            *(u32x2*)(out + (size_t)q * ldo + p) = w;
        }
}

__global__ __launch_bounds__(256, 2) void k1(const bf16_t* __restrict__ x, const bf16_t* __restrict__ wre,
                                            const bf16_t* __restrict__ wim, bf16_t* __restrict__ AB) {
    extern __shared__ __attribute__((aligned(16))) unsigned char shm[];
    const int b = blockIdx.x;
    const int st = b & 15, et = b >> 4;
    const int im = et >> 3, e0 = (et & 7) * 128, s0 = st * 128;
    const bf16_t* W = im ? wim : wre;
    gemm_tile<128>(x + (size_t)s0 * D, x, D, W + (size_t)e0 * D, W, D, D, D,
                   AB + (size_t)e0 * 4096 + im * 2048 + s0, 4096, im ? -1.f : 1.f, shm);
}

__global__ __launch_bounds__(256, 2) void k2(const bf16_t* __restrict__ AB, const bf16_t* __restrict__ sre,
                                            const bf16_t* __restrict__ sim, bf16_t* __restrict__ y) {
    extern __shared__ __attribute__((aligned(16))) unsigned char shm[];
    const int b = blockIdx.x;
    const int et = b & 7, tt = b >> 3;
    const int e0 = et * 128, t0 = tt * 64;
    gemm_tile<64>(AB + (size_t)e0 * 4096, AB + (size_t)e0 * 4096 + 2048, 4096,
                  sre + (size_t)t0 * S, sim + (size_t)t0 * S, S, 2048, 4096,
                  y + (size_t)t0 * D + e0, D, 1.f, shm);
}

extern "C" void kernel_launch(void* const* d_in, const int* in_sizes, int n_in,
                              void* d_out, int out_size, void* d_ws, size_t ws_size,
                              hipStream_t stream) {
    const bf16_t* x = (const bf16_t*)d_in[0];
    const bf16_t* sre = (const bf16_t*)d_in[1];
    const bf16_t* sim = (const bf16_t*)d_in[2];
    const bf16_t* wre = (const bf16_t*)d_in[3];
    const bf16_t* wim = (const bf16_t*)d_in[4];
    bf16_t* AB = (bf16_t*)d_ws;
    bf16_t* y = (bf16_t*)d_out;
    k1<<<256, 256, 65536, stream>>>(x, wre, wim, AB);
    k2<<<256, 256, 49152, stream>>>(AB, sre, sim, y);
}
```

```cpp
#include <hip/hip_runtime.h>
#include <hip/hip_bf16.h>

typedef unsigned short bf16_t;
typedef short bf16x8 __attribute__((ext_vector_type(8)));
typedef float f32x4 __attribute__((ext_vector_type(4)));
typedef unsigned u32x2 __attribute__((ext_vector_type(2)));

constexpr int S = 2048, D = 1024;

__device__ __forceinline__ unsigned cvt_pk_bf16(float lo, float hi) {
    typedef __bf16 bf16x2_t __attribute__((ext_vector_type(2)));
    typedef float f32x2_t __attribute__((ext_vector_type(2)));
    bf16x2_t r = __builtin_convertvector((f32x2_t){lo, hi}, bf16x2_t);
    return __builtin_bit_cast(unsigned, r);
}

template <int BQ>
__device__ __forceinline__ void gemm_tile(const bf16_t* __restrict__ P0, const bf16_t* __restrict__ P1, int ldp,
                                          const bf16_t* __restrict__ Q0, const bf16_t* __restrict__ Q1, int ldq,
                                          int khalf, int ktot, bf16_t* __restrict__ out, int ldo, float sign,
                                          unsigned char* shm) {
    const int tid = threadIdx.x, wid = tid >> 6, lane = tid & 63, wr = wid >> 1, wc = wid & 1, fr = lane & 15, fq = lane >> 4;
    constexpr int BK = 64;
    constexpr int PB = 128 * BK * 2, QB = BQ * BK * 2;
    constexpr int NQ = BQ / 32;
    f32x4 acc[4][NQ];
#pragma unroll
    for (int i = 0; i < 4; ++i)
#pragma unroll
        for (int j = 0; j < NQ; ++j) acc[i][j] = (f32x4){0.f, 0.f, 0.f, 0.f};
    auto stage = [&](int buf, int kt) {
        const int k0 = kt * BK;
        const bf16_t* Pb = k0 < khalf ? P0 + k0 : P1 + (k0 - khalf);
        const bf16_t* Qb = k0 < khalf ? Q0 + k0 : Q1 + (k0 - khalf);
        unsigned char* sp = shm + buf * (PB + QB);
        unsigned char* sq = sp + PB;
#pragma unroll
        for (int i = 0; i < PB / 4096; ++i) {
            const int b = tid * 16 + i * 4096, r = b >> 7, c = (b >> 4) & 7, cs = c ^ (r & 7);
            __builtin_amdgcn_global_load_lds((const unsigned*)(Pb + (size_t)r * ldp + cs * 8), (unsigned*)(sp + b), 16, 0, 0);
        }
#pragma unroll
        for (int i = 0; i < QB / 4096; ++i) {
            const int b = tid * 16 + i * 4096, r = b >> 7, c = (b >> 4) & 7, cs = c ^ (r & 7);
            __builtin_amdgcn_global_load_lds((const unsigned*)(Qb + (size_t)r * ldq + cs * 8), (unsigned*)(sq + b), 16, 0, 0);
        }
    };
    const int nt = ktot / BK;
    stage(0, 0);
    asm volatile("s_waitcnt vmcnt(0)" ::: "memory");
    __syncthreads();
    for (int t = 0; t < nt; ++t) {
        const int cur = t & 1;
        if (t + 1 < nt) stage(cur ^ 1, t + 1);
        const unsigned char* sp = shm + cur * (PB + QB);
        const unsigned char* sq = sp + PB;
#pragma unroll
        for (int kk = 0; kk < 2; ++kk) {
            bf16x8 a[4], b[NQ];
#pragma unroll
            for (int i = 0; i < 4; ++i) {
                const int r = wr * 64 + i * 16 + fr, c = (kk * 4 + fq) ^ (r & 7);
                a[i] = *(const bf16x8*)(sp + r * 128 + c * 16);
            }
#pragma unroll
            for (int j = 0; j < NQ; ++j) {
                const int r = wc * (BQ / 2) + j * 16 + fr, c = (kk * 4 + fq) ^ (r & 7);
                b[j] = *(const bf16x8*)(sq + r * 128 + c * 16);
            }
#pragma unroll
            for (int i = 0; i < 4; ++i)
#pragma unroll
                for (int j = 0; j < NQ; ++j)
                    acc[i][j] = __builtin_amdgcn_mfma_f32_16x16x32_bf16(a[i], b[j], acc[i][j], 0, 0, 0);
        }
        asm volatile("s_waitcnt vmcnt(0)" ::: "memory");
        __syncthreads();
    }
#pragma unroll
    for (int i = 0; i < 4; ++i)
#pragma unroll
        for (int j = 0; j < NQ; ++j) {
            const int p = wr * 64 + i * 16 + fq * 4, q = wc * (BQ / 2) + j * 16 + fr;
            u32x2 w;
            w.x = cvt_pk_bf16(acc[i][j][0] * sign, acc[i][j][1] * sign);
            w.y = cvt_pk_bf16(acc[i][j][2] * sign, acc[i][j][3] * sign);
            *(u32x2*)(out + (size_t)q * ldo + p) = w;
        }
}

__global__ __launch_bounds__(256, 2) void k1(const bf16_t* __restrict__ x, const bf16_t* __restrict__ wre,
                                            const bf16_t* __restrict__ wim, bf16_t* __restrict__ AB) {
    extern __shared__ __attribute__((aligned(16))) unsigned char shm[];
    const int b = blockIdx.x;
    const int st = b & 15, et = b >> 4;
    const int im = et >> 3, e0 = (et & 7) * 128, s0 = st * 128;
    const bf16_t* W = im ? wim : wre;
    gemm_tile<128>(x + (size_t)s0 * D, x, D, W + (size_t)e0 * D, W, D, D, D,
                   AB + (size_t)e0 * 4096 + im * 2048 + s0, 4096, im ? -1.f : 1.f, shm);
}

__global__ __launch_bounds__(256, 2) void k2(const bf16_t* __restrict__ AB, const bf16_t* __restrict__ sre,
                                            const bf16_t* __restrict__ sim, bf16_t* __restrict__ y) {
    extern __shared__ __attribute__((aligned(16))) unsigned char shm[];
    const int b = blockIdx.x;
    const int et = b & 7, tt = b >> 3;
    const int e0 = et * 128, t0 = tt * 64;
    gemm_tile<64>(AB + (size_t)e0 * 4096, AB + (size_t)e0 * 4096 + 2048, 4096,
                  sre + (size_t)t0 * S, sim + (size_t)t0 * S, S, 2048, 4096,
                  y + (size_t)t0 * D + e0, D, 1.f, shm);
}

__global__ __launch_bounds__(256) void k_touch(const uint4* __restrict__ a, int na, const uint4* __restrict__ b, int nb, const uint4* __restrict__ c, int nc,
                        const uint4* __restrict__ d, int nd, const uint4* __restrict__ e, int ne, unsigned* sink) {
    unsigned acc = 0;
    const int i0 = blockIdx.x * 256 + threadIdx.x, st = gridDim.x * 256;
    for (int i = i0; i < na; i += st) { uint4 v = a[i]; acc ^= v.x ^ v.y ^ v.z ^ v.w; }
    for (int i = i0; i < nb; i += st) { uint4 v = b[i]; acc ^= v.x ^ v.y ^ v.z ^ v.w; }
    for (int i = i0; i < nc; i += st) { uint4 v = c[i]; acc ^= v.x ^ v.y ^ v.z ^ v.w; }
    for (int i = i0; i < nd; i += st) { uint4 v = d[i]; acc ^= v.x ^ v.y ^ v.z ^ v.w; }
    for (int i = i0; i < ne; i += st) { uint4 v = e[i]; acc ^= v.x ^ v.y ^ v.z ^ v.w; }
    if (acc == 0x12345678u) sink[0] = acc;
}

extern "C" void kernel_launch(void* const* d_in, const int* in_sizes, int n_in,
                              void* d_out, int out_size, void* d_ws, size_t ws_size,
                              hipStream_t stream) {
    const bf16_t* x = (const bf16_t*)d_in[0];
    const bf16_t* sre = (const bf16_t*)d_in[1];
    const bf16_t* sim = (const bf16_t*)d_in[2];
    const bf16_t* wre = (const bf16_t*)d_in[3];
    const bf16_t* wim = (const bf16_t*)d_in[4];
    bf16_t* AB = (bf16_t*)d_ws;
    bf16_t* y = (bf16_t*)d_out;
    k_touch<<<1024, 256, 0, stream>>>((const uint4*)x, S * D / 8, (const uint4*)wre, D * D / 8, (const uint4*)wim, D * D / 8, (const uint4*)sre, S * S / 8, (const uint4*)sim, S * S / 8, (unsigned*)((char*)d_ws + (64 << 20)));
    k1<<<256, 256, 65536, stream>>>(x, wre, wim, AB);
    k2<<<256, 256, 49152, stream>>>(AB, sre, sim, y);
}
```

```cpp
#include <hip/hip_runtime.h>
#include <hip/hip_bf16.h>

typedef unsigned short bf16_t;
typedef short bf16x8 __attribute__((ext_vector_type(8)));
typedef float f32x4 __attribute__((ext_vector_type(4)));
typedef unsigned u32x4 __attribute__((ext_vector_type(4)));
#define LAS __attribute__((address_space(3)))

constexpr int S = 2048, D = 1024;
constexpr int NS = 4;
constexpr int SLOT = 32768, HALF = 16384;
constexpr unsigned AB_BYTES = 2u * S * D * 2;
constexpr unsigned MB_OFF = AB_BYTES;
constexpr unsigned MB_BYTES = 256u * 16384;
constexpr unsigned FL_OFF = MB_OFF + MB_BYTES;
constexpr unsigned EP_OFF = FL_OFF + 4096;
constexpr unsigned WS_USED = EP_OFF + 256;
constexpr unsigned SPIN_MAX = 1u << 18;

#define STAMP(i)
#define STAMP0(i)
#define TRACE_BYTES 0
#define LBAR() bar()

__device__ __forceinline__ unsigned cvt_pk_bf16(float lo, float hi) {
    typedef __bf16 bf16x2_t __attribute__((ext_vector_type(2)));
    typedef float f32x2_t __attribute__((ext_vector_type(2)));
    bf16x2_t r = __builtin_convertvector((f32x2_t){lo, hi}, bf16x2_t);
    return __builtin_bit_cast(unsigned, r);
}
__device__ __forceinline__ u32x4 pack8(const f32x4& a, const f32x4& b) {
    u32x4 v; v.x = cvt_pk_bf16(a[0], a[1]); v.y = cvt_pk_bf16(a[2], a[3]); v.z = cvt_pk_bf16(b[0], b[1]); v.w = cvt_pk_bf16(b[2], b[3]); return v;
}
template <int N> __device__ __forceinline__ void wait_vm() { asm volatile("s_waitcnt vmcnt(%0)" :: "n"(N) : "memory"); }
__device__ __forceinline__ void wait_lgkm0() { asm volatile("" ::: "memory"); __builtin_amdgcn_s_waitcnt(0xc07f); asm volatile("" ::: "memory"); }
__device__ __forceinline__ void bar() { asm volatile("" ::: "memory"); __builtin_amdgcn_s_barrier(); asm volatile("" ::: "memory"); }
__device__ __forceinline__ __amdgpu_buffer_rsrc_t mk_rsrc(const void* p, unsigned bytes) {
    return __builtin_amdgcn_make_buffer_rsrc((void*)p, (short)0, (int)bytes, 0x00020000);
}
__device__ __forceinline__ int perm32(int r) { return (r & ~31) | ((r & 12) << 1) | ((r & 16) >> 2) | (r & 3); }
__device__ __forceinline__ int korder(int p, int cls, int nt) { return ((p & 3) == cls) ? ((p + 4) & (nt - 1)) : p; }
__device__ __forceinline__ float bf_lo(unsigned d) { return __uint_as_float(d << 16); }
__device__ __forceinline__ float bf_hi(unsigned d) { return __uint_as_float(d & 0xffff0000u); }
__device__ __forceinline__ unsigned add_bf2(unsigned a, unsigned b) { return cvt_pk_bf16(bf_lo(a) + bf_lo(b), bf_hi(a) + bf_hi(b)); }

struct Frags { bf16x8 a[4][2], b[4][2]; };

__device__ __forceinline__ void read_frags(Frags& f, const LAS unsigned char* slot, const int (&pa)[2], const int (&qa)[2]) {
#pragma unroll
    for (int kk = 0; kk < 2; ++kk) f.a[0][kk] = *(const LAS bf16x8*)(slot + pa[kk]);
#pragma unroll
    for (int j = 0; j < 4; ++j)
#pragma unroll
        for (int kk = 0; kk < 2; ++kk) f.b[j][kk] = *(const LAS bf16x8*)(slot + qa[kk] + j * 2048);
#pragma unroll
    for (int i = 1; i < 4; ++i)
#pragma unroll
        for (int kk = 0; kk < 2; ++kk) f.a[i][kk] = *(const LAS bf16x8*)(slot + pa[kk] + i * 2048);
}
__device__ __forceinline__ void mma(f32x4 (&acc)[4][4], const Frags& f) {
#pragma unroll
    for (int i = 0; i < 4; ++i)
#pragma unroll
        for (int j = 0; j < 4; ++j)
#pragma unroll
            for (int kk = 0; kk < 2; ++kk)
                acc[i][j] = __builtin_amdgcn_mfma_f32_16x16x32_bf16(f.a[i][kk], f.b[j][kk], acc[i][j], 0, 0, 0);
}
__device__ __forceinline__ void interleave() {
#pragma unroll
    for (int i = 0; i < 8; ++i) {
        __builtin_amdgcn_sched_group_barrier(0x008, 1, 0);
        __builtin_amdgcn_sched_group_barrier(0x100, 1, 0);
        __builtin_amdgcn_sched_group_barrier(0x008, 1, 0);
        __builtin_amdgcn_sched_group_barrier(0x100, 1, 0);
        __builtin_amdgcn_sched_group_barrier(0x008, 1, 0);
    }
    __builtin_amdgcn_sched_group_barrier(0x008, 8, 0);
}

__device__ __forceinline__ void consume(f32x4 (&acc)[4][4], LAS unsigned char* shm, const int (&pa)[2], const int (&qa)[2], int p0, int n) {
    Frags f0, f1;
    bar();
    read_frags(f0, shm + (p0 & (NS - 1)) * SLOT, pa, qa);
#pragma unroll 1
    for (int k = p0 + 1; k < p0 + n - 1; k += 2) {
        wait_lgkm0(); bar();
        __builtin_amdgcn_sched_barrier(0);
        read_frags(f1, shm + (k & (NS - 1)) * SLOT, pa, qa);
        mma(acc, f0);
        interleave();
        __builtin_amdgcn_sched_barrier(0);
        wait_lgkm0(); bar();
        __builtin_amdgcn_sched_barrier(0);
        read_frags(f0, shm + ((k + 1) & (NS - 1)) * SLOT, pa, qa);
        mma(acc, f1);
        interleave();
        __builtin_amdgcn_sched_barrier(0);
    }
    wait_lgkm0(); bar();
    __builtin_amdgcn_sched_barrier(0);
    read_frags(f1, shm + ((p0 + n - 1) & (NS - 1)) * SLOT, pa, qa);
    mma(acc, f0);
    interleave();
    __builtin_amdgcn_sched_barrier(0);
    mma(acc, f1);
}

template <int AUX>
__device__ __forceinline__ void issue4(__amdgpu_buffer_rsrc_t rs, LAS unsigned char* dst, const int (&v)[4], int soff) {
#pragma unroll
    for (int i = 0; i < 4; ++i) __builtin_amdgcn_raw_ptr_buffer_load_lds(rs, dst + i * 1024, 16, v[i], soff, 0, AUX);
}

__global__ __launch_bounds__(512, 2) void fnet_fused(const bf16_t* __restrict__ x, const bf16_t* __restrict__ sre, const bf16_t* __restrict__ sim,
                                                    const bf16_t* __restrict__ wre, const bf16_t* __restrict__ wim,
                                                    unsigned char* __restrict__ ws, bf16_t* __restrict__ y) {
    __shared__ __attribute__((aligned(16))) unsigned char shm_[NS * SLOT + 16 + TRACE_BYTES];
    LAS unsigned char* shm = (LAS unsigned char*)shm_;
    volatile LAS unsigned* ctl = (volatile LAS unsigned*)(shm + NS * SLOT);
    const int tid = threadIdx.x, lane = tid & 63;
    const int w = __builtin_amdgcn_readfirstlane(tid >> 6);
    const int b = blockIdx.x, xcd = b & 7, rr = b >> 3;
    const int eb = (xcd & 1) * 4 + (rr & 3), m = ((xcd >> 1) & 1) * 8 + (rr >> 2), im = xcd >> 2, cls = (eb + m) & 3;
    const int grp = eb * 2 + im;
    STAMP0(0);
    const __amdgpu_buffer_rsrc_t rws = mk_rsrc(ws, WS_USED);

    if (w >= 4) {
        const int lw = w - 4;
        LAS unsigned char* dst0 = shm + lw * 4096;
        int v1[4], vq[4], vp[4];
#pragma unroll
        for (int i = 0; i < 4; ++i) {
            const int rho = lw * 32 + i * 8 + (lane >> 3);
            const int c = ((lane & 7) ^ ((rho >> 1) & 7)) * 16;
            v1[i] = perm32(rho) * (D * 2) + c;
            vq[i] = rho * (S * 2) + c;
            vp[i] = lw * 4096 + i * 1024 + lane * 16;
        }
        {
            const __amdgpu_buffer_rsrc_t rx = mk_rsrc(x, S * D * 2);
            const __amdgpu_buffer_rsrc_t rw = mk_rsrc(im ? wim : wre, D * D * 2);
            const int pbase = m * 128 * D * 2, qbase = eb * 128 * D * 2;
#pragma unroll 1
            for (int pos = 0; pos < 16; ++pos) {
                if (pos >= NS - 1) { wait_vm<8 * (NS - 2)>(); LBAR(); }
                const int kt = korder(pos, cls, 16);
                LAS unsigned char* dst = dst0 + (pos & (NS - 1)) * SLOT;
                issue4<0>(rx, dst, v1, pbase + kt * 128);
                issue4<0>(rw, dst + HALF, v1, qbase + kt * 128);
            }
        }
        const __amdgpu_buffer_rsrc_t rq = mk_rsrc(im ? sim : sre, S * S * 2);
        const int pbase = grp * (512 * 1024), qbase = m * 128 * S * 2;
        const int b0 = korder(0, cls, 32), b1 = korder(1, cls, 32);
        auto k2 = [&](int j) {
            if (j < 2) return 2 * m + j;
            const int k = korder(j, cls, 32);
            return k == 2 * m ? b0 : (k == 2 * m + 1 ? b1 : k);
        };
        wait_vm<16>(); LBAR();
        issue4<0>(rq, dst0 + 0 * SLOT + HALF, vq, qbase + k2(0) * 128);
        wait_vm<12>(); LBAR();
        issue4<0>(rq, dst0 + 1 * SLOT + HALF, vq, qbase + k2(1) * 128);
        wait_vm<8>(); LBAR();
        issue4<0>(rq, dst0 + 2 * SLOT + HALF, vq, qbase + k2(2) * 128);
        wait_vm<8>(); LBAR();
        issue4<0>(rq, dst0 + 3 * SLOT + HALF, vq, qbase + k2(3) * 128);
        wait_vm<8>(); LBAR();
        issue4<0>(rq, dst0 + 0 * SLOT + HALF, vq, qbase + k2(4) * 128);
        if (lw == 0) {
            const unsigned want = ctl[0];
            const unsigned char* fp = ws + FL_OFF + grp * 256 + lane * 4;
            const unsigned char* pp = ws + FL_OFF + (grp ^ 1) * 256 + m * 16 + (lane & 3) * 4;
            for (unsigned spins = 0; spins < SPIN_MAX; ++spins) {
                unsigned f0, f1;
                asm volatile("global_load_dword %0, %2, off sc1\n\tglobal_load_dword %1, %3, off sc1\n\ts_waitcnt vmcnt(0)"
                             : "=&v"(f0), "=&v"(f1) : "v"(fp), "v"(pp) : "memory");
                if (__all(f0 == want && f1 == want)) break;
                __builtin_amdgcn_s_sleep(2);
            }
            ctl[1] = 1u;
            wait_lgkm0();
            STAMP(6);
        } else {
            for (unsigned spins = 0; spins < (SPIN_MAX << 4); ++spins) {
                if (ctl[1] == 1u) break;
                __builtin_amdgcn_s_sleep(1);
            }
        }
        asm volatile("" ::: "memory");
        issue4<16>(rws, dst0 + 2 * SLOT, vp, pbase + k2(2) * HALF);
        issue4<16>(rws, dst0 + 3 * SLOT, vp, pbase + k2(3) * HALF);
        issue4<16>(rws, dst0 + 0 * SLOT, vp, pbase + k2(4) * HALF);
        wait_vm<8>(); LBAR();
        {
            LAS unsigned char* dst = dst0 + 1 * SLOT; const int kt = k2(5);
            issue4<16>(rws, dst, vp, pbase + kt * HALF); issue4<0>(rq, dst + HALF, vq, qbase + kt * 128);
        }
        wait_vm<12>(); LBAR();
        {
            LAS unsigned char* dst = dst0 + 2 * SLOT; const int kt = k2(6);
            issue4<16>(rws, dst, vp, pbase + kt * HALF); issue4<0>(rq, dst + HALF, vq, qbase + kt * 128);
        }
#pragma unroll 1
        for (int j = 7; j < 32; ++j) {
            wait_vm<16>(); LBAR();
            LAS unsigned char* dst = dst0 + (j & (NS - 1)) * SLOT; const int kt = k2(j);
            issue4<16>(rws, dst, vp, pbase + kt * HALF); issue4<0>(rq, dst + HALF, vq, qbase + kt * 128);
        }
        wait_vm<16>(); LBAR();
        wait_vm<8>(); LBAR();
        wait_vm<0>(); LBAR();
        if (b == 0 && lw == 0) {
            const unsigned want = ctl[0];
            for (unsigned spins = 0; spins < SPIN_MAX; ++spins) {
                bool ok = true;
#pragma unroll
                for (int i = 0; i < 4; ++i) {
                    const u32x4 f = __builtin_amdgcn_raw_buffer_load_b128(rws, FL_OFF + i * 1024 + lane * 16, 0, 16);
                    ok = ok && f.x == want && f.y == want && f.z == want && f.w == want;
                }
                if (__all(ok)) break;
                __builtin_amdgcn_s_sleep(2);
            }
            if (lane == 0) __builtin_amdgcn_raw_buffer_store_b32(want, rws, EP_OFF, 0, 16);
        }
        return;
    }

    const int wp = w & 1, wq = w >> 1, r = lane & 15, q = lane >> 4, sw = (r >> 1) & 7;
    const unsigned tag = __builtin_amdgcn_raw_buffer_load_b32(rws, EP_OFF, 0, 16) + 1u;
    const int mine = MB_OFF + b * 16384 + w * 4096 + lane * 16, theirs = MB_OFF + (b ^ 4) * 16384 + w * 4096 + lane * 16;
#pragma unroll
    for (int i = 0; i < 4; ++i) __builtin_amdgcn_raw_buffer_store_b128((u32x4){~0u, ~0u, ~0u, ~0u}, rws, mine + i * 1024, 0, 16);
    if (w == 0 && lane == 0) { ctl[0] = tag; ctl[1] = 0u; }
    int pa[2], qa[2];
#pragma unroll
    for (int kk = 0; kk < 2; ++kk) {
        const int c = ((4 * kk + q) ^ sw) * 16;
        pa[kk] = (wp * 64 + r) * 128 + c;
        qa[kk] = HALF + (wq * 64 + r) * 128 + c;
    }
    f32x4 acc[4][4];
#pragma unroll
    for (int i = 0; i < 4; ++i)
#pragma unroll
        for (int j = 0; j < 4; ++j) acc[i][j] = (f32x4){0.f, 0.f, 0.f, 0.f};

#pragma unroll 1
    for (int seg = 0; seg < 3; ++seg) {
        const int p0 = seg == 0 ? 0 : (seg == 1 ? 16 : 18), n = seg == 0 ? 16 : (seg == 1 ? 2 : 30);
        consume(acc, shm, pa, qa, p0, n);
        if (seg == 0) {
            STAMP0(2);
            const unsigned sgn = im ? 0x80008000u : 0u;
            const int off = wq * 8192 + lane * 16;
            const int goff = grp * (512 * 1024) + (m * 2 + wp) * HALF + off;
#pragma unroll
            for (int j = 0; j < 4; ++j)
#pragma unroll
                for (int p = 0; p < 2; ++p) {
                    const u32x4 v = pack8(acc[2 * p][j], acc[2 * p + 1][j]) ^ sgn;
                    __builtin_amdgcn_raw_buffer_store_b128(v, rws, goff + j * 2048 + p * 1024, 0, 16);
                    *(LAS u32x4*)(shm + wp * SLOT + off + j * 2048 + p * 1024) = v;
                }
#pragma unroll
            for (int i = 0; i < 4; ++i)
#pragma unroll
                for (int j = 0; j < 4; ++j) acc[i][j] = (f32x4){0.f, 0.f, 0.f, 0.f};
            pa[0] = wp * 8192 + lane * 16; pa[1] = pa[0] + 1024;
            wait_lgkm0();
            STAMP0(3);
        } else if (seg == 1) {
            STAMP0(4);
            wait_vm<0>();
            if (lane == 0) __builtin_amdgcn_raw_buffer_store_b32(tag, rws, FL_OFF + grp * 256 + m * 16 + w * 4, 0, 16);
            STAMP0(5);
        }
    }
    STAMP0(8);
    u32x4 keep[2][2];
#pragma unroll
    for (int jj = 0; jj < 2; ++jj)
#pragma unroll
        for (int p = 0; p < 2; ++p) {
            const u32x4 v0 = pack8(acc[2 * p][jj], acc[2 * p + 1][jj]), v1 = pack8(acc[2 * p][2 + jj], acc[2 * p + 1][2 + jj]);
            u32x4 snd = im ? v0 : v1;
            keep[jj][p] = im ? v1 : v0;
            snd = __builtin_elementwise_min(snd, (u32x4){0xfffffffeu, 0xfffffffeu, 0xfffffffeu, 0xfffffffeu});
            __builtin_amdgcn_raw_buffer_store_b128(snd, rws, theirs + (jj * 2 + p) * 1024, 0, 16);
        }
    STAMP0(9);
    u32x4 rcv[2][2];
    for (unsigned spins = 0;; ++spins) {
        bool ok = true;
#pragma unroll
        for (int jj = 0; jj < 2; ++jj)
#pragma unroll
            for (int p = 0; p < 2; ++p) {
                rcv[jj][p] = __builtin_amdgcn_raw_buffer_load_b128(rws, mine + (jj * 2 + p) * 1024, 0, 16);
                ok = ok && rcv[jj][p].x != ~0u && rcv[jj][p].y != ~0u && rcv[jj][p].z != ~0u && rcv[jj][p].w != ~0u;
            }
        if (__all(ok) || spins > SPIN_MAX) break;
        __builtin_amdgcn_s_sleep(1);
    }
    STAMP0(10);
    bf16_t* yb = y + (size_t)(m * 128 + wq * 64 + im * 32 + r) * D + eb * 128 + wp * 64 + q * 8;
#pragma unroll
    for (int jj = 0; jj < 2; ++jj)
#pragma unroll
        for (int p = 0; p < 2; ++p) {
            u32x4 o;
            o.x = add_bf2(keep[jj][p].x, rcv[jj][p].x); o.y = add_bf2(keep[jj][p].y, rcv[jj][p].y);
            o.z = add_bf2(keep[jj][p].z, rcv[jj][p].z); o.w = add_bf2(keep[jj][p].w, rcv[jj][p].w);
            *(u32x4*)(yb + (size_t)jj * 16 * D + p * 32) = o;
        }
    STAMP0(11);
}

extern "C" void kernel_launch(void* const* d_in, const int* in_sizes, int n_in,
                              void* d_out, int out_size, void* d_ws, size_t ws_size,
                              hipStream_t stream) {
    fnet_fused<<<256, 512, 0, stream>>>((const bf16_t*)d_in[0], (const bf16_t*)d_in[1], (const bf16_t*)d_in[2],
                                        (const bf16_t*)d_in[3], (const bf16_t*)d_in[4], (unsigned char*)d_ws, (bf16_t*)d_out);
}
```

```cpp
#define ALLPRI
#define Q18EARLY
#define YAUX 16
#include <hip/hip_runtime.h>
#include <hip/hip_bf16.h>

typedef unsigned short bf16_t;
typedef short bf16x8 __attribute__((ext_vector_type(8)));
typedef unsigned u32x4 __attribute__((ext_vector_type(4)));
typedef unsigned u32x2 __attribute__((ext_vector_type(2)));
#define LAS __attribute__((address_space(3)))

constexpr int S = 2048, D = 1024;
constexpr int HALF = 16384, SLOT = 32768, NS = 4;
constexpr int CTL = NS * SLOT;
constexpr unsigned AB_BYTES = 2u * S * D * 2;
constexpr unsigned MB_OFF = AB_BYTES;
constexpr unsigned MB_BYTES = 128u * 32768;
constexpr unsigned FL_OFF = MB_OFF + MB_BYTES;
constexpr unsigned EP_OFF = FL_OFF + 4096;
constexpr unsigned WS_USED = EP_OFF + 256;
constexpr unsigned SPIN_MAX = 1u << 18;
#define CODE_BYTES 11264
#define DELTA 2
#define RD_END 24
#define FLAG_AT 21
#define ORD1_0 0xfedba9c765832140ull
#define ORD1_1 0xfecbad8769432501ull
#define ORD1_2 0xfdcbe987a5436102ull
#define ORD1_3 0xedcfa98b65472103ull
#define ORD1_PHI 0x4444444444444444ull

#define STAMP(i)
#define STAMP0(i)
#define LBAR() bar()

__device__ __forceinline__ unsigned cvt_pk_bf16(float lo, float hi) {
    typedef __bf16 bf16x2_t __attribute__((ext_vector_type(2)));
    typedef float f32x2_t __attribute__((ext_vector_type(2)));
    bf16x2_t r = __builtin_convertvector((f32x2_t){lo, hi}, bf16x2_t);
    return __builtin_bit_cast(unsigned, r);
}
template <int N> __device__ __forceinline__ void wait_vm() { asm volatile("s_waitcnt vmcnt(%0)" :: "n"(N) : "memory"); }
__device__ __forceinline__ void wait_lgkm0() { asm volatile("" ::: "memory"); __builtin_amdgcn_s_waitcnt(0xc07f); asm volatile("" ::: "memory"); }
__device__ __forceinline__ void bar() { asm volatile("s_barrier" ::: "memory"); }
__device__ __forceinline__ __amdgpu_buffer_rsrc_t mk_rsrc(const void* p, unsigned bytes) {
    return __builtin_amdgcn_make_buffer_rsrc((void*)p, (short)0, (int)bytes, 0x00020000);
}
__device__ __forceinline__ int perm32(int r) { return (r & ~31) | ((r & 12) << 1) | ((r & 16) >> 2) | (r & 3); }
__device__ __forceinline__ float bf_lo(unsigned d) { return __uint_as_float(d << 16); }
__device__ __forceinline__ float bf_hi(unsigned d) { return __uint_as_float(d & 0xffff0000u); }
__device__ __forceinline__ unsigned lds_rd32(int addr) { unsigned v; asm volatile("ds_read_b32 %0, %1\n\ts_waitcnt lgkmcnt(0)" : "=v"(v) : "v"(addr) : "memory"); return v; }
__device__ __forceinline__ u32x2 lds_rd64(int addr) { u32x2 v; asm volatile("ds_read_b64 %0, %1\n\ts_waitcnt lgkmcnt(0)" : "=v"(v) : "v"(addr) : "memory"); return v; }
__device__ __forceinline__ void lds_wr32(int addr, unsigned v) { asm volatile("ds_write_b32 %0, %1\n\ts_waitcnt lgkmcnt(0)" :: "v"(addr), "v"(v) : "memory"); }

#define ACLOB "a0","a1","a2","a3","a4","a5","a6","a7","a8","a9","a10","a11","a12","a13","a14","a15","a16","a17","a18","a19","a20","a21","a22","a23","a24","a25","a26","a27","a28","a29","a30","a31","a32","a33","a34","a35","a36","a37","a38","a39","a40","a41","a42","a43","a44","a45","a46","a47","a48","a49","a50","a51","a52","a53","a54","a55","a56","a57","a58","a59","a60","a61","a62","a63","a64","a65","a66","a67","a68","a69","a70","a71","a72","a73","a74","a75","a76","a77","a78","a79","a80","a81","a82","a83","a84","a85","a86","a87","a88","a89","a90","a91","a92","a93","a94","a95","a96","a97","a98","a99","a100","a101","a102","a103","a104","a105","a106","a107","a108","a109","a110","a111","a112","a113","a114","a115","a116","a117","a118","a119","a120","a121","a122","a123","a124","a125","a126","a127"
struct Frags { bf16x8 a[4][2], b[4][2]; };
constexpr int ACC(int i, int j) { return (i * 4 + j) * 4; }
constexpr int FA(int i, int kk) { return 64 + (i * 2 + kk) * 4; }
constexpr int FB(int j, int kk) { return 96 + (j * 2 + kk) * 4; }
template <int R, bool TOA> __device__ __forceinline__ void rd(Frags& f, const int (&p)[2], const int (&q)[2]) {
    constexpr int kk = R & 1, t = R >> 1;
    constexpr bool isa = t == 0 || t > 4;
    constexpr int idx = t == 0 ? 0 : (t <= 4 ? t - 1 : t - 4);
    const int addr = isa ? p[kk] : q[kk];
    if constexpr (TOA) {
        constexpr int r = isa ? FA(idx, kk) : FB(idx, kk);
        asm volatile("ds_read_b128 a[%c1:%c2], %0 offset:%c3" :: "v"(addr), "i"(r), "i"(r + 3), "i"(idx * 2048 + SLOT));
    } else if constexpr (isa) asm volatile("ds_read_b128 %0, %1 offset:%c2" : "=v"(f.a[idx][kk]) : "v"(addr), "i"(idx * 2048));
    else asm volatile("ds_read_b128 %0, %1 offset:%c2" : "=v"(f.b[idx][kk]) : "v"(addr), "i"(idx * 2048));
}
template <int M, bool FROMA> __device__ __forceinline__ void mm(const Frags& f) {
    constexpr int kk = M & 1, j = (M >> 1) & 3, i = M >> 3, c = ACC(i, j);
    if constexpr (FROMA) asm volatile("v_mfma_f32_16x16x32_bf16 a[%c0:%c1], a[%c2:%c3], a[%c4:%c5], a[%c0:%c1]" :: "i"(c), "i"(c + 3), "i"(FA(i, kk)), "i"(FA(i, kk) + 3), "i"(FB(j, kk)), "i"(FB(j, kk) + 3));
    else asm volatile("v_mfma_f32_16x16x32_bf16 a[%c2:%c3], %0, %1, a[%c2:%c3]" :: "v"(f.a[i][kk]), "v"(f.b[j][kk]), "i"(c), "i"(c + 3));
}
template <int M0, int N, bool FROMA> __device__ __forceinline__ void mms(const Frags& f) {
    if constexpr (N > 0) { mm<M0, FROMA>(f); mms<M0 + 1, N - 1, FROMA>(f); }
}
template <int R0, int N> __device__ __forceinline__ void rds(Frags& f, const int (&p)[2], const int (&q)[2]) {
    if constexpr (N > 0) { rd<R0, false>(f, p, q); rds<R0 + 1, N - 1>(f, p, q); }
}
template <int M, bool TOA> __device__ __forceinline__ void mmrd(Frags& f, const int (&p)[2], const int (&q)[2]) {
    if constexpr (M < 32) {
        mm<M, !TOA>(f);
        constexpr int r0 = (M * 16 + RD_END - 1) / RD_END, r1 = ((M + 1) * 16 + RD_END - 1) / RD_END;
        if constexpr (M < RD_END && r1 > r0) rd<r0, TOA>(f, p, q);
        mmrd<M + 1, TOA>(f, p, q);
    }
}
template <int R> __device__ __forceinline__ void accrd(float (&o)[64]) { asm volatile("v_accvgpr_read_b32 %0, a[%c1]" : "=v"(o[R]) : "i"(R)); }
template <int R0, int N> __device__ __forceinline__ void accrds(float (&o)[64]) { if constexpr (N > 0) { accrd<R0>(o); accrds<R0 + 1, N - 1>(o); } }
template <int M> __device__ __forceinline__ void mmout(const Frags& f, float (&o)[64]) {
    if constexpr (M < 32) { mm<M, true>(f); if constexpr (M >= 9) accrds<2 * (M - 9), 2>(o); mmout<M + 1>(f, o); }
    else accrds<46, 18>(o);
}
template <int R> __device__ __forceinline__ void acczero() { if constexpr (R < 64) { asm volatile("v_accvgpr_write_b32 a[%c0], 0" :: "i"(R)); acczero<R + 1>(); } }
__device__ __forceinline__ void lgkm_bar() { asm volatile("s_waitcnt lgkmcnt(0)\n\ts_barrier" ::: "memory"); }

template <int AUX>
__device__ __forceinline__ void issue4(__amdgpu_buffer_rsrc_t rs, LAS unsigned char* dst, const int (&v)[4], int soff) {
    __builtin_amdgcn_raw_ptr_buffer_load_lds(rs, dst, 16, v[0], soff, 0, AUX);
    __builtin_amdgcn_raw_ptr_buffer_load_lds(rs, dst, 16, v[1], soff, 1024, AUX);
    __builtin_amdgcn_raw_ptr_buffer_load_lds(rs, dst, 16, v[2], soff, 2048, AUX);
    __builtin_amdgcn_raw_ptr_buffer_load_lds(rs, dst, 16, v[3], soff, 3072, AUX);
}

__device__ __forceinline__ int lf_order(int p, int c, int n) {
    const int g = (p - 1) >> 2, o = (p - 1) & 3;
    const bool last = p >= n - 3;
    const int i = last ? o : o - (o > c);
    int v = g * 4 + i + (i >= c);
    if (!last && o == c) v = (g + 1) * 4 + c;
    return p == 0 ? c : v;
}

extern "C" __global__ __launch_bounds__(512, 2) void fnet_fused(const bf16_t* __restrict__ x, const bf16_t* __restrict__ sre, const bf16_t* __restrict__ sim,
                                                               const bf16_t* __restrict__ wre, const bf16_t* __restrict__ wim,
                                                               unsigned char* __restrict__ ws, bf16_t* __restrict__ y) {
    __shared__ __attribute__((aligned(16))) unsigned char shm_[CTL + 2560];
    LAS unsigned char* shm = (LAS unsigned char*)shm_;
    const int lbase = (int)(unsigned)(size_t)shm, ctl = lbase + CTL;
    const int tid = threadIdx.x, lane = tid & 63;
    const int w = __builtin_amdgcn_readfirstlane(tid >> 6);
    const int b = blockIdx.x, xcd = b & 7, rr = b >> 3;
    const int xe = xcd & 1, xt = (xcd >> 1) & 1, im = xcd >> 2;
    const int eb = xe * 4 + (rr & 3), m = xt * 8 + (rr >> 2), cls = (eb + m) & 3, phi = xe ^ xt;
    const int grp = eb * 2 + im;
    const bool late = ((m ^ im) & 1) != 0;
    const int pend = late ? 48 + DELTA : 48 - DELTA;
    const bool pri = true;
    const int mbox = MB_OFF + (eb * 16 + m) * 32768;
    const __amdgpu_buffer_rsrc_t rws = mk_rsrc(ws, WS_USED);

    if (__builtin_expect(w >= 4, 1)) {
        const int lw = w - 4;
        LAS unsigned char* dst0 = shm + lw * 4096;
        __amdgpu_buffer_rsrc_t rp = mk_rsrc(x, S * D * 2);
        __amdgpu_buffer_rsrc_t rq = mk_rsrc(im ? wim : wre, D * D * 2);
        int vp[4], vq[4];
#pragma unroll
        for (int i = 0; i < 4; ++i) {
            const int rho = lw * 32 + i * 8 + (lane >> 3);
            vp[i] = vq[i] = perm32(rho) * (D * 2) + ((lane & 7) ^ ((rho >> 1) & 7)) * 16 - i * 1024;
        }
        {
            const int k0 = (cls ^ (phi << 2)) * 128;
            issue4<16>(rp, dst0, vp, m * 128 * D * 2 + k0);
            issue4<0>(rq, dst0 + HALF, vq, eb * 128 * D * 2 + k0);
        }
        int tabp, tabq;
        {
            const unsigned long long ord1 = (cls == 0 ? ORD1_0 : cls == 1 ? ORD1_1 : cls == 2 ? ORD1_2 : ORD1_3) ^ (phi ? ORD1_PHI : 0ull);
            int v = lf_order(lane & 31, cls, 32); if (v < 24) v ^= phi << 2;
            const int u = v >> 1, blk = (u >> 1) + 8 * (u & 1), kk = 2 * blk + (v & 1);
            const int sk0 = m == 15 ? (DELTA == 2 ? 7 : 14) : 15, sk1 = DELTA == 2 ? sk0 : (m == 7 ? 14 : 7);
            const bool keep = lane < 32 && blk != m && (late || DELTA == 0 || (blk != sk0 && blk != sk1));
            const unsigned long long bal = __ballot(keep);
            const int posn = keep ? 18 + __builtin_amdgcn_mbcnt_hi((unsigned)(bal >> 32), __builtin_amdgcn_mbcnt_lo((unsigned)bal, 0)) : 63;
            int k = __builtin_amdgcn_ds_permute(posn * 4, kk);
            if (lane < 18) k = 2 * m + (lane & 1);
            if (lane >= 48) k = 2 * ((lane & 2) ? sk1 : sk0) + (lane & 1);
            tabp = (lane >= 48 ? grp ^ 1 : grp) * (512 * 1024) + k * HALF;
            tabq = m * 128 * S * 2 + k * 128;
            if (lane < 16) { const int k1 = (int)((ord1 >> (4 * lane)) & 15) * 128; tabp = m * 128 * D * 2 + k1; tabq = eb * 128 * D * 2 + k1; }
        }
        auto issP = [&](int pos) { issue4<16>(rp, dst0 + (pos & (NS - 1)) * SLOT, vp, __builtin_amdgcn_readlane(tabp, pos)); };
        auto issQ = [&](int pos) { issue4<0>(rq, dst0 + (pos & (NS - 1)) * SLOT + HALF, vq, __builtin_amdgcn_readlane(tabq, pos)); };
        unsigned want = 0;
        const unsigned char* fp = ws + FL_OFF + eb * 512 + lane * 8;
        auto poll = [&](bool need) {
#pragma unroll 1
            for (unsigned spins = 0; spins < SPIN_MAX; ++spins) {
                u32x2 f;
                asm volatile("global_load_dwordx2 %0, %1, off sc1\n\ts_waitcnt vmcnt(0)" : "=&v"(f) : "v"(fp) : "memory");
                if (__all(!need || (f.x == want && f.y == want))) break;
                __builtin_amdgcn_s_sleep(1);
            }
        };
        issP(1); issQ(1);
        int pos = 2;
#pragma unroll 1
        for (int sub = 0; sub < 5; ++sub) {
            int send = 16;
            if (sub == 1) {
                rp = rws; rq = mk_rsrc(im ? sim : sre, S * S * 2);
#pragma unroll
                for (int i = 0; i < 4; ++i) {
                    const int rho = lw * 32 + i * 8 + (lane >> 3);
                    vq[i] = rho * (S * 2) + ((lane & 7) ^ ((rho >> 1) & 7)) * 16 - i * 1024;
                    vp[i] = lw * 4096 + lane * 16;
                }
                issQ(16); wait_vm<12>(); LBAR();
                issQ(17); wait_vm<8>(); LBAR();
                issQ(18); wait_vm<8>(); LBAR();
                wait_vm<4>(); LBAR();
                want = lds_rd32(ctl);
                if (lw == 0) {
                    poll((lane >> 5) == im && (lane & 8) == 0);
                    lds_wr32(ctl + 4, 1u);
                    STAMP(6);
                } else {
#pragma unroll 1
                    for (unsigned spins = 0; spins < (SPIN_MAX << 4); ++spins) {
                        if (__builtin_amdgcn_readfirstlane(lds_rd32(ctl + 4)) == 1u) break;
                        __builtin_amdgcn_s_sleep(1);
                    }
                }
                issP(18); issP(19); issQ(19);
                wait_vm<8>(); LBAR();
                issP(20); issQ(20);
                pos = 21; send = 27;
            } else if (sub == 2) {
                __builtin_amdgcn_raw_ptr_buffer_load_lds(rws, shm + CTL + 256 + lw * 512, 4, lane * 4, FL_OFF + eb * 512, 0, 16);
                __builtin_amdgcn_raw_ptr_buffer_load_lds(rws, shm + CTL + 256 + lw * 512, 4, lane * 4, FL_OFF + eb * 512, 256, 16);
                send = 29;
            } else if (sub == 3) {
                const u32x2 f = lds_rd64(ctl + 256 + lw * 512 + lane * 8);
                if (!__all(f.x == want && f.y == want)) poll(true);
                send = late ? 48 : pend;
            } else if (sub == 4) {
                rq = mk_rsrc(im ? sre : sim, S * S * 2);
                send = pend;
            }
#pragma unroll 1
            for (; pos < send; ++pos) {
                issP(pos); issQ(pos);
                wait_vm<16>(); LBAR();
            }
        }
        wait_vm<8>(); LBAR();
        if (late) {
            LAS unsigned char* dst = dst0 + (pend & (NS - 1)) * SLOT;
            issue4<16>(rws, dst, vp, mbox); issue4<16>(rws, dst + HALF, vp, mbox + HALF);
            wait_vm<8>(); LBAR();
            wait_vm<0>(); bar();
        } else { wait_vm<0>(); LBAR(); }
        if (b == 0 && lw == 0) {
#pragma unroll 1
            for (unsigned spins = 0; spins < SPIN_MAX; ++spins) {
                bool ok = true;
#pragma unroll
                for (int i = 0; i < 4; ++i) {
                    const u32x4 f = __builtin_amdgcn_raw_buffer_load_b128(rws, FL_OFF + i * 1024 + lane * 16, 0, 16);
                    ok = ok && f.x == want && f.y == want && f.z == want && f.w == want;
                }
                if (__all(ok)) break;
                __builtin_amdgcn_s_sleep(2);
            }
            if (lane == 0) __builtin_amdgcn_raw_buffer_store_b32(want, rws, EP_OFF, 0, 16);
        }
        return;
    }

    STAMP0(0);
#define PAD(i)
    const int wp = w & 1, wq = w >> 1, r = lane & 15, q = lane >> 4, sw = (r >> 1) & 7;
    if (w == 0) {
        unsigned long long pc; unsigned back;
        asm volatile("s_getpc_b64 %0\n.Lfnet_pc:\n\ts_mov_b32 %1, .Lfnet_pc-fnet_fused" : "=s"(pc), "=s"(back));
        const __amdgpu_buffer_rsrc_t rc = mk_rsrc((const void*)(pc - back), CODE_BYTES);
        if (lane < 8) __builtin_amdgcn_raw_ptr_buffer_load_lds(rc, shm + CTL + 2304, 4, (lane * 32 + rr) * 128, 0, 0, 0);
    }
    const unsigned tag = __builtin_amdgcn_raw_buffer_load_b32(rws, EP_OFF, 0, 16) + 1u;
#pragma unroll
    for (int i = 0; i < 4; ++i) __builtin_amdgcn_raw_buffer_store_b128((u32x4){~0u, ~0u, ~0u, ~0u}, rws, w * 4096 + i * 1024 + lane * 16, mbox + im * HALF, 16);
    int pa[2], qa[2];
#pragma unroll
    for (int kk = 0; kk < 2; ++kk) {
        const int c = ((4 * kk + q) ^ sw) * 16;
        pa[kk] = lbase + (wp * 64 + r) * 128 + c;
        qa[kk] = lbase + HALF + (wq * 64 + r) * 128 + c;
    }
    asm volatile("" ::: ACLOB);
    acczero<0>();
    if (w == 0) lds_wr32(ctl + (lane & 1) * 4, (lane & 1) ? 0u : tag);
    float o[64];
    auto pk = [&](int p, int j) { u32x4 v; const float* a = &o[(2 * p * 4 + j) * 4]; const float* c = &o[((2 * p + 1) * 4 + j) * 4];
        v.x = cvt_pk_bf16(a[0], a[1]); v.y = cvt_pk_bf16(a[2], a[3]); v.z = cvt_pk_bf16(c[0], c[1]); v.w = cvt_pk_bf16(c[2], c[3]); return v; };
    auto send_flag = [&]() {
        wait_vm<0>();
        if (lane == 0) __builtin_amdgcn_raw_buffer_store_b32(tag, rws, FL_OFF + grp * 256 + m * 16 + w * 4, 0, 16);
    };
    const int off = wq * 8192 + lane * 16;
    const int goff = grp * (512 * 1024) + (m * 2 + wp) * HALF;
    const int flag_at = pri ? -1 : FLAG_AT;
    Frags f;
    bar();
    STAMP0(1);
    PAD(1);
#pragma unroll 1
    for (int seg = 0; seg < 3; ++seg) {
        const int p0 = seg == 0 ? 0 : (seg == 1 ? 16 : 18), p1 = seg == 0 ? 16 : (seg == 1 ? 18 : pend);
        int p[2], qq[2];
        { const int so = (p0 & 2) * SLOT; p[0] = pa[0] + so; p[1] = pa[1] + so; qq[0] = qa[0] + so; qq[1] = qa[1] + so; }
        rds<0, 16>(f, p, qq);
#pragma unroll 1
        for (int k = p0 + 1;; k += 2) {
            lgkm_bar();
            mmrd<0, true>(f, p, qq);
            if (k + 1 == p1) break;
            p[0] ^= 2 * SLOT; p[1] ^= 2 * SLOT; qq[0] ^= 2 * SLOT; qq[1] ^= 2 * SLOT;
            if (__builtin_expect(k == flag_at, 0)) { send_flag(); STAMP0(5); }
            lgkm_bar();
            mmrd<0, false>(f, p, qq);
        }
        asm volatile("s_waitcnt lgkmcnt(0)" ::: "memory");
        if (seg == 2) break;
        if (seg == 0) {
            mmout<0>(f, o);
            STAMP0(2);
            PAD(2);
            const unsigned sgn = im ? 0x80008000u : 0u;
#pragma unroll
            for (int j = 0; j < 4; ++j)
#pragma unroll
                for (int pp = 0; pp < 2; ++pp) {
                    const u32x4 v = pk(pp, j) ^ sgn;
                    if (pri) __builtin_amdgcn_raw_buffer_store_b128(v, rws, off + j * 2048 + pp * 1024, goff, 16);
                    *(LAS u32x4*)(shm + wp * SLOT + off + j * 2048 + pp * 1024) = v;
                }
            acczero<0>();
            pa[0] = lbase + wp * 8192 + lane * 16; pa[1] = pa[0] + 1024;
            if (pri) { send_flag(); STAMP0(4); }
            wait_lgkm0();
            bar();
        } else {
            mms<0, 32, true>(f);
            if (!pri) {
                u32x4 v[8];
#pragma unroll
                for (int i = 0; i < 8; ++i) v[i] = *(const LAS u32x4*)(shm + wp * SLOT + off + i * 1024);
                wait_lgkm0();
                bar();
#pragma unroll
                for (int i = 0; i < 8; ++i) __builtin_amdgcn_raw_buffer_store_b128(v[i], rws, off + i * 1024, goff, 16);
            } else bar();
            PAD(3);
        }
    }
    mmout<0>(f, o);
    PAD(4);
    const int moff = w * 8192 + lane * 16;
    if (!late) {
        STAMP0(8);
#pragma unroll
        for (int j = 0; j < 4; ++j)
#pragma unroll
            for (int pp = 0; pp < 2; ++pp) {
                const u32x4 snd = __builtin_elementwise_min(pk(pp, j), (u32x4){0xfffffffeu, 0xfffffffeu, 0xfffffffeu, 0xfffffffeu});
                __builtin_amdgcn_raw_buffer_store_b128(snd, rws, moff + (j * 2 + pp) * 1024, mbox, 16);
            }
        STAMP0(9);
        PAD(5);
        return;
    }
    STAMP0(10);
    bar();
    STAMP0(11);
    const LAS unsigned char* src = shm + (pend & (NS - 1)) * SLOT + moff;
    const __amdgpu_buffer_rsrc_t ry = mk_rsrc(y, S * D * 2);
    const int yoff = ((m * 128 + wq * 64 + r) * D + eb * 128 + wp * 64 + q * 8) * 2;
#pragma unroll
    for (int hj = 0; hj < 2; ++hj) {
        u32x4 rcv[4];
        bool ok = true;
#pragma unroll
        for (int i = 0; i < 4; ++i) {
            rcv[i] = *(const LAS u32x4*)(src + (hj * 4 + i) * 1024);
            ok = ok && rcv[i].x != ~0u && rcv[i].y != ~0u && rcv[i].z != ~0u && rcv[i].w != ~0u;
        }
#pragma unroll 1
        for (unsigned spins = 0; !__all(ok) && spins < SPIN_MAX; ++spins) {
            ok = true;
#pragma unroll
            for (int i = 0; i < 4; ++i) {
                rcv[i] = __builtin_amdgcn_raw_buffer_load_b128(rws, moff + (hj * 4 + i) * 1024, mbox, 16);
                ok = ok && rcv[i].x != ~0u && rcv[i].y != ~0u && rcv[i].z != ~0u && rcv[i].w != ~0u;
            }
        }
#pragma unroll
        for (int i = 0; i < 4; ++i) {
            const int j = hj * 2 + (i >> 1), pp = i & 1;
            const u32x4 h = rcv[i];
            float* a = &o[(2 * pp * 4 + j) * 4]; float* c = &o[((2 * pp + 1) * 4 + j) * 4];
            a[0] += bf_lo(h.x); a[1] += bf_hi(h.x); a[2] += bf_lo(h.y); a[3] += bf_hi(h.y);
            c[0] += bf_lo(h.z); c[1] += bf_hi(h.z); c[2] += bf_lo(h.w); c[3] += bf_hi(h.w);
            __builtin_amdgcn_raw_buffer_store_b128(pk(pp, j), ry, yoff + j * 16 * D * 2 + pp * 64, 0, YAUX);
        }
        asm volatile("" ::: "memory");
    }
    STAMP0(12);
    PAD(5);
}

extern "C" void kernel_launch(void* const* d_in, const int* in_sizes, int n_in,
                              void* d_out, int out_size, void* d_ws, size_t ws_size,
                              hipStream_t stream) {
    fnet_fused<<<256, 512, 0, stream>>>((const bf16_t*)d_in[0], (const bf16_t*)d_in[1], (const bf16_t*)d_in[2],
                                        (const bf16_t*)d_in[3], (const bf16_t*)d_in[4], (unsigned char*)d_ws, (bf16_t*)d_out);
}
```

```cpp
#include <hip/hip_runtime.h>
#include <hip/hip_bf16.h>

typedef unsigned short bf16_t;
typedef short bf16x8 __attribute__((ext_vector_type(8)));
typedef unsigned u32x4 __attribute__((ext_vector_type(4)));
#define LAS __attribute__((address_space(3)))

constexpr int S = 2048, D = 1024;
constexpr int HALF = 16384, SLOT = 32768, NS = 4;
constexpr int CTL = NS * SLOT;
constexpr unsigned AB_BYTES = 2u * S * D * 2;
constexpr unsigned MB_OFF = AB_BYTES;
constexpr unsigned MB_BYTES = 128u * 32768;
constexpr unsigned FL_OFF = MB_OFF + MB_BYTES;
constexpr unsigned F2_OFF = FL_OFF + 32768;
constexpr unsigned EP_OFF = F2_OFF + 4096;
constexpr unsigned WS_USED = EP_OFF + 256;
constexpr unsigned SPIN_MAX = 1u << 18;
constexpr int SC1 = 16;
constexpr int RD_END = 24;
constexpr int NPOS = 46;
constexpr int ST2 = 29;

template <int N> __device__ __forceinline__ void wait_vm() { asm volatile("s_waitcnt vmcnt(%0)" :: "n"(N) : "memory"); }
__device__ __forceinline__ void wait_lgkm0() { asm volatile("" ::: "memory"); __builtin_amdgcn_s_waitcnt(0xc07f); asm volatile("" ::: "memory"); }
__device__ __forceinline__ void bar() { asm volatile("s_barrier" ::: "memory"); }
__device__ __forceinline__ void lgkm() { asm volatile("s_waitcnt lgkmcnt(0)" ::: "memory"); }
__device__ __forceinline__ void lgkm_bar() { asm volatile("s_waitcnt lgkmcnt(0)\n\ts_barrier" ::: "memory"); }
__device__ __forceinline__ __amdgpu_buffer_rsrc_t mk_rsrc(const void* p, unsigned bytes) {
    return __builtin_amdgcn_make_buffer_rsrc((void*)p, (short)0, (int)bytes, 0x00020000);
}
__device__ __forceinline__ int perm32(int r) { return (r & ~31) | ((r & 12) << 1) | ((r & 16) >> 2) | (r & 3); }
__device__ __forceinline__ unsigned lds_rd32(int addr) { unsigned v; asm volatile("ds_read_b32 %0, %1\n\ts_waitcnt lgkmcnt(0)" : "=v"(v) : "v"(addr) : "memory"); return v; }
__device__ __forceinline__ void lds_wr32(int addr, unsigned v) { asm volatile("ds_write_b32 %0, %1\n\ts_waitcnt lgkmcnt(0)" :: "v"(addr), "v"(v) : "memory"); }

#define ACLOB "a0","a1","a2","a3","a4","a5","a6","a7","a8","a9","a10","a11","a12","a13","a14","a15","a16","a17","a18","a19","a20","a21","a22","a23","a24","a25","a26","a27","a28","a29","a30","a31","a32","a33","a34","a35","a36","a37","a38","a39","a40","a41","a42","a43","a44","a45","a46","a47","a48","a49","a50","a51","a52","a53","a54","a55","a56","a57","a58","a59","a60","a61","a62","a63","a64","a65","a66","a67","a68","a69","a70","a71","a72","a73","a74","a75","a76","a77","a78","a79","a80","a81","a82","a83","a84","a85","a86","a87","a88","a89","a90","a91","a92","a93","a94","a95","a96","a97","a98","a99","a100","a101","a102","a103","a104","a105","a106","a107","a108","a109","a110","a111","a112","a113","a114","a115","a116","a117","a118","a119","a120","a121","a122","a123","a124","a125","a126","a127"
struct Frags { bf16x8 a[4][2], b[4][2]; };
constexpr int ACC(int i, int j) { return (i * 4 + j) * 4; }
constexpr int FA(int i, int kk) { return 64 + (i * 2 + kk) * 4; }
constexpr int FB(int j, int kk) { return 96 + (j * 2 + kk) * 4; }
template <int R> __device__ __forceinline__ void acczero() { if constexpr (R < 64) { asm volatile("v_accvgpr_write_b32 a[%c0], 0" :: "i"(R)); acczero<R + 1>(); } }
template <int R, bool TOA> __device__ __forceinline__ void rd(Frags& f, const int (&p)[2], const int (&q)[2]) {
    constexpr int kk = R & 1, t = R >> 1;
    constexpr bool isa = t == 0 || t > 4;
    constexpr int idx = t == 0 ? 0 : (t <= 4 ? t - 1 : t - 4);
    const int addr = isa ? p[kk] : q[kk];
    if constexpr (TOA) {
        constexpr int r = isa ? FA(idx, kk) : FB(idx, kk);
        asm volatile("ds_read_b128 a[%c1:%c2], %0 offset:%c3" :: "v"(addr), "i"(r), "i"(r + 3), "i"(idx * 2048 + SLOT));
    } else if constexpr (isa) asm volatile("ds_read_b128 %0, %1 offset:%c2" : "=v"(f.a[idx][kk]) : "v"(addr), "i"(idx * 2048));
    else asm volatile("ds_read_b128 %0, %1 offset:%c2" : "=v"(f.b[idx][kk]) : "v"(addr), "i"(idx * 2048));
}
template <int I, int J, int KK, bool FROMA> __device__ __forceinline__ void mm1(const Frags& f) {
    constexpr int c = ACC(I, J);
    if constexpr (FROMA) asm volatile("v_mfma_f32_16x16x32_bf16 a[%c0:%c1], a[%c2:%c3], a[%c4:%c5], a[%c0:%c1]" :: "i"(c), "i"(c + 3), "i"(FA(I, KK)), "i"(FA(I, KK) + 3), "i"(FB(J, KK)), "i"(FB(J, KK) + 3));
    else asm volatile("v_mfma_f32_16x16x32_bf16 a[%c2:%c3], %0, %1, a[%c2:%c3]" :: "v"(f.a[I][KK]), "v"(f.b[J][KK]), "i"(c), "i"(c + 3));
}
template <int R> __device__ __forceinline__ void rds(Frags& f, const int (&p)[2], const int (&q)[2]) {
    if constexpr (R < 16) { rd<R, false>(f, p, q); rds<R + 1>(f, p, q); }
}
template <int M, bool TOA> __device__ __forceinline__ void mmrd(Frags& f, const int (&p)[2], const int (&q)[2]) {
    if constexpr (M < 32) {
        mm1<(M >> 3), (((M >> 3) & 1) ? 3 - ((M >> 1) & 3) : ((M >> 1) & 3)), ((M ^ (M >> 1)) & 1), !TOA>(f);
        constexpr int r0 = (M * 16 + RD_END - 1) / RD_END, r1 = ((M + 1) * 16 + RD_END - 1) / RD_END;
        if constexpr (M < RD_END && r1 > r0) rd<r0, TOA>(f, p, q);
        mmrd<M + 1, TOA>(f, p, q);
    }
}
template <int M, bool TOA> __device__ __forceinline__ void mmrdh(Frags& f, const int (&p)[2], const int (&q)[2]) {
    if constexpr (M < 16) {
        mm1<(M >> 2), ((M >> 1) & 1), (M & 1), !TOA>(f);
        if constexpr (M < 12) rd<(M < 6 ? M : M + 4), TOA>(f, p, q);
        mmrdh<M + 1, TOA>(f, p, q);
    }
}
template <int J, int JEND> __device__ __forceinline__ void mmcols(const Frags& f) {
    if constexpr (J < JEND) {
        mm1<0, J, 0, true>(f); mm1<0, J, 1, true>(f); mm1<1, J, 0, true>(f); mm1<1, J, 1, true>(f);
        mm1<2, J, 0, true>(f); mm1<2, J, 1, true>(f); mm1<3, J, 0, true>(f); mm1<3, J, 1, true>(f);
        mmcols<J + 1, JEND>(f);
    }
}
template <int R> __device__ __forceinline__ unsigned accpk2() {
    unsigned d; float t;
    asm volatile("v_accvgpr_read_b32 %0, a[%c2]\n\tv_accvgpr_read_b32 %1, a[%c3]\n\tv_cvt_pk_bf16_f32 %0, %0, %1" : "=&v"(d), "=&v"(t) : "i"(R), "i"(R + 1));
    return d;
}
template <int C, int C0, class F> __device__ __forceinline__ void tail1(const Frags& f, F&& out) {
    if constexpr (C < 10) {
        constexpr int J = C >> 1, P = C & 1, O = C - 2, OJ = O >> 1, OP = O & 1;
        u32x4 v;
        if constexpr (C < 8) mm1<2 * P, J, 0, true>(f);
        if constexpr (O >= C0) v.x = accpk2<ACC(2 * OP, OJ)>();
        if constexpr (C < 8) mm1<2 * P, J, 1, true>(f);
        if constexpr (O >= C0) v.y = accpk2<ACC(2 * OP, OJ) + 2>();
        if constexpr (C < 8) mm1<2 * P + 1, J, 0, true>(f);
        if constexpr (O >= C0) v.z = accpk2<ACC(2 * OP + 1, OJ)>();
        if constexpr (C < 8) mm1<2 * P + 1, J, 1, true>(f);
        if constexpr (O >= C0) { v.w = accpk2<ACC(2 * OP + 1, OJ) + 2>(); out(v, O); asm volatile("" ::: "memory"); }
        if constexpr (C == C0 + 1) asm volatile("s_nop 3");
        tail1<C + 1, C0>(f, out);
    }
}
template <int C, class F> __device__ __forceinline__ void tail2(const u32x4 (&h)[4], const u32x4& sel0, const u32x4& sel1, F&& out) {
    if constexpr (C < 7) {
        constexpr int J = C >> 1, P = C & 1, O = C - 3, OJ = O >> 1, OP = O & 1;
        u32x4 v;
        if constexpr (C < 4) asm volatile("v_mfma_f32_16x16x32_bf16 a[%c2:%c3], %0, %1, a[%c2:%c3]" :: "v"(sel0), "v"(h[C & 3]), "i"(ACC(2 * P, J)), "i"(ACC(2 * P, J) + 3));
        if constexpr (C >= 3) { v.x = accpk2<ACC(2 * OP, OJ)>(); v.y = accpk2<ACC(2 * OP, OJ) + 2>(); }
        if constexpr (C < 4) asm volatile("v_mfma_f32_16x16x32_bf16 a[%c2:%c3], %0, %1, a[%c2:%c3]" :: "v"(sel1), "v"(h[C & 3]), "i"(ACC(2 * P + 1, J)), "i"(ACC(2 * P + 1, J) + 3));
        if constexpr (C >= 3) { v.z = accpk2<ACC(2 * OP + 1, OJ)>(); v.w = accpk2<ACC(2 * OP + 1, OJ) + 2>(); out(v, O); asm volatile("" ::: "memory"); }
        if constexpr (C == 2) asm volatile("s_nop 3");
        tail2<C + 1>(h, sel0, sel1, out);
    }
}

template <int AUX>
__device__ __forceinline__ void issue4(__amdgpu_buffer_rsrc_t rs, LAS unsigned char* dst, const int (&v)[4], int soff) {
    __builtin_amdgcn_raw_ptr_buffer_load_lds(rs, dst, 16, v[0], soff, 0, AUX);
    __builtin_amdgcn_raw_ptr_buffer_load_lds(rs, dst, 16, v[1], soff, 1024, AUX);
    __builtin_amdgcn_raw_ptr_buffer_load_lds(rs, dst, 16, v[2], soff, 2048, AUX);
    __builtin_amdgcn_raw_ptr_buffer_load_lds(rs, dst, 16, v[3], soff, 3072, AUX);
}

__device__ __forceinline__ int lf_order(int p, int c, int n) {
    const int g = (p - 1) >> 2, o = (p - 1) & 3;
    const bool last = p >= n - 3;
    const int i = last ? o : o - (o > c);
    int v = g * 4 + i + (i >= c);
    if (!last && o == c) v = (g + 1) * 4 + c;
    return p == 0 ? c : v;
}

extern "C" __global__ __launch_bounds__(512, 2) void fnet_fused(const bf16_t* __restrict__ x, const bf16_t* __restrict__ sre, const bf16_t* __restrict__ sim,
                                                               const bf16_t* __restrict__ wre, const bf16_t* __restrict__ wim,
                                                               unsigned char* __restrict__ ws, bf16_t* __restrict__ y) {
    __shared__ __attribute__((aligned(16))) unsigned char shm_[CTL + 1280];
    LAS unsigned char* shm = (LAS unsigned char*)shm_;
    const int lbase = (int)(unsigned)(size_t)shm, ctl = lbase + CTL;
    const int tid = threadIdx.x, lane = tid & 63;
    const int w = __builtin_amdgcn_readfirstlane(tid >> 6);
    const int b = blockIdx.x, xcd = b & 7, rr = b >> 3;
    const int xe = xcd & 1, xt = (xcd >> 1) & 1, im = xcd >> 2;
    const int eb = xe * 4 + (rr & 3), m = xt * 8 + (rr >> 2), cls = (eb + m) & 3, phi = xe ^ xt;
    const int grp = eb * 2 + im;
    const int mbox = MB_OFF + (eb * 16 + m) * 32768;
    const __amdgpu_buffer_rsrc_t rws = mk_rsrc(ws, WS_USED);

    if (__builtin_expect(w >= 4, 1)) {
        const int lw = w - 4;
        LAS unsigned char* dst0 = shm + lw * 4096;
        __amdgpu_buffer_rsrc_t rp = mk_rsrc(x, S * D * 2);
        __amdgpu_buffer_rsrc_t rq = mk_rsrc(im ? wim : wre, D * D * 2);
        int vp[4], vq[4];
#pragma unroll
        for (int i = 0; i < 4; ++i) {
            const int rho = lw * 32 + i * 8 + (lane >> 3);
            vp[i] = vq[i] = perm32(rho) * (D * 2) + ((lane & 7) ^ ((rho >> 1) & 7)) * 16 - i * 1024;
        }
        {
            const int k0 = (cls ^ (phi << 2)) * 128;
            issue4<SC1>(rp, dst0, vp, m * 128 * D * 2 + k0);
            issue4<0>(rq, dst0 + HALF, vq, eb * 128 * D * 2 + k0);
        }
        int tabp, tabq;
        {
            const int v = lf_order(lane & 31, cls, 32) ^ (phi << 2);
            const int u = v >> 1, blk = (u >> 1) + 8 * (u & 1), kk = 2 * blk + (v & 1);
            const int sk = m == 15 ? 7 : 15;
            const bool keep = lane < 32 && blk != m && blk != sk;
            const unsigned long long bal = __ballot(keep);
            const int posn = keep ? 18 + __builtin_amdgcn_mbcnt_hi((unsigned)(bal >> 32), __builtin_amdgcn_mbcnt_lo((unsigned)bal, 0)) : 63;
            int k = __builtin_amdgcn_ds_permute(posn * 4, kk);
            if (lane < 18) k = 2 * m + (lane & 1);
            if (lane >= NPOS) k = 2 * sk + (lane & 1);
            tabp = (lane >= NPOS + 2 ? grp ^ 1 : grp) * (512 * 1024) + k * HALF;
            tabq = m * 128 * S * 2 + k * 128;
            if (lane < 16) { const int k1 = (lf_order(lane, cls, 16) ^ (phi << 2)) * 128; tabp = m * 128 * D * 2 + k1; tabq = eb * 128 * D * 2 + k1; }
        }
        auto issP = [&](int pos) { issue4<SC1>(rp, dst0 + (pos & (NS - 1)) * SLOT, vp, __builtin_amdgcn_readlane(tabp, pos)); };
        auto issQ = [&](int pos) { issue4<0>(rq, dst0 + (pos & (NS - 1)) * SLOT + HALF, vq, __builtin_amdgcn_readlane(tabq, pos)); };
        unsigned want = 0;
        auto poll = [&](const unsigned char* fp, bool need) {
#pragma unroll 1
            for (unsigned spins = 0; spins < SPIN_MAX; ++spins) {
                unsigned f;
                asm volatile("global_load_dword %0, %1, off sc1\n\ts_waitcnt vmcnt(0)" : "=&v"(f) : "v"(fp) : "memory");
                if (__all(!need || f == want)) break;
                __builtin_amdgcn_s_sleep(1);
            }
        };
        const unsigned char* fp = ws + FL_OFF + (grp * 16 + m) * 128 + (lane & 31) * 4;
        issP(1); issQ(1);
        wait_vm<8>(); bar();
        issP(2); issQ(2);
        int pos = 3;
        auto run = [&](int to) {
#pragma unroll 1
            for (; pos < to; ++pos) {
                issP(pos); issQ(pos);
                wait_vm<16>(); bar();
            }
        };
        const __amdgpu_buffer_rsrc_t rs1 = mk_rsrc(im ? sim : sre, S * S * 2), rs2 = mk_rsrc(im ? sre : sim, S * S * 2);
        run(16);
        rp = rws; rq = rs1;
#pragma unroll
        for (int i = 0; i < 4; ++i) {
            const int rho = lw * 32 + i * 8 + (lane >> 3);
            vq[i] = (rho ^ (im << 5)) * (S * 2) + ((lane & 7) ^ ((rho >> 1) & 7)) * 16 - i * 1024;
            vp[i] = lw * 4096 + lane * 16;
        }
        issQ(16);
        if (lw < 2) {
            __builtin_amdgcn_raw_ptr_buffer_load_lds(rq, shm + CTL + 1024, 4, (lw * 64 + lane) * (S * 2), __builtin_amdgcn_readlane(tabq, 18), 0, 0);
            wait_vm<13>(); bar();
            issQ(17); wait_vm<9>(); bar();
            wait_vm<5>();
        } else {
            wait_vm<12>(); bar();
            issQ(17); wait_vm<8>(); bar();
            wait_vm<4>();
        }
        want = lds_rd32(ctl);
        bar();
        issQ(18); issQ(19); wait_vm<8>();
        bar();
        if (lw == 0) {
            poll(fp, ((lane >> 4) & 1) == im && (lane & 4) == 0);
            lds_wr32(ctl + 4, 1u);
        } else {
#pragma unroll 1
            for (unsigned spins = 0; spins < (SPIN_MAX << 4); ++spins) {
                if (__builtin_amdgcn_readfirstlane(lds_rd32(ctl + 4)) == 1u) break;
                __builtin_amdgcn_s_sleep(1);
            }
        }
        issP(18); issP(19); wait_vm<4>();
        bar();
        issP(20); issQ(20);
        pos = 21;
        run(ST2 - 2);
        if (lw == 0) __builtin_amdgcn_raw_ptr_buffer_load_lds(rws, shm + CTL + 256, 4, (lane & 31) * 4, FL_OFF + (grp * 16 + m) * 128, 0, SC1);
        run(ST2);
        if (!__all(lds_rd32(ctl + 256 + lane * 4) == want)) poll(fp, true);
        if (lw == 1) {
            const int row = ((eb & 2) << 5) + (im << 5) + (eb & 1) * 16 + ((lane >> 1) & 15), vo = row * (S * 2) + (lane & 1) * 128;
            if (lane < 32) __builtin_amdgcn_raw_ptr_buffer_load_lds(rs1, shm + CTL + 1024, 4, vo, __builtin_amdgcn_readlane(tabq, NPOS), 0, 0);
            else __builtin_amdgcn_raw_ptr_buffer_load_lds(rs2, shm + CTL + 1024, 4, vo, __builtin_amdgcn_readlane(tabq, NPOS), 0, 0);
        }
        run(NPOS);
        if (lw == 0) __builtin_amdgcn_raw_ptr_buffer_load_lds(rws, shm + CTL + 768, 4, (lane & 3) * 4, F2_OFF + ((grp ^ 1) * 16 + m) * 16, 0, SC1);
        int vh[2];
#pragma unroll
        for (int i = 0; i < 2; ++i) {
            const int rho = (lw >> 1) * 64 + (lw & 1) * 16 + i * 8 + (lane >> 3);
            vh[i] = (rho ^ (im << 5)) * (S * 2) + ((lane & 7) ^ ((rho >> 1) & 7)) * 16 - i * 1024;
        }
        auto issH = [&](int p, __amdgpu_buffer_rsrc_t r) {
            issP(p);
            LAS unsigned char* dst = shm + (p & (NS - 1)) * SLOT + HALF + (lw >> 1) * 8192 + (lw & 1) * 2048;
            const int so = __builtin_amdgcn_readlane(tabq, p);
            __builtin_amdgcn_raw_ptr_buffer_load_lds(r, dst, 16, vh[0], so, 0, 0);
            __builtin_amdgcn_raw_ptr_buffer_load_lds(r, dst, 16, vh[1], so, 1024, 0);
        };
        issH(46, rs1); wait_vm<14>(); bar();
        issH(47, rs1); wait_vm<12>(); bar();
        issH(48, rs2); wait_vm<12>(); bar();
        issH(49, rs2); wait_vm<12>(); bar();
        if (!__all(lds_rd32(ctl + 768 + lane * 4) == want)) poll(ws + F2_OFF + ((grp ^ 1) * 16 + m) * 16 + (lane & 3) * 4, true);
        wait_vm<6>(); bar();
        wait_vm<0>(); bar();
        if (b == 0 && lw == 0) {
#pragma unroll 1
            for (unsigned spins = 0; spins < SPIN_MAX; ++spins) {
                asm volatile("" ::: "memory");
                const u32x4 f = __builtin_amdgcn_raw_buffer_load_b128(rws, FL_OFF + (lane >> 3) * 4096 + (lane & 7) * 16, 0, SC1);
                if (__all(f.x == want && f.y == want && f.z == want && f.w == want)) break;
                __builtin_amdgcn_s_sleep(2);
            }
            if (lane == 0) __builtin_amdgcn_raw_buffer_store_b32(want, rws, EP_OFF, 0, SC1);
        }
        return;
    }

    const int wp = w & 1, wq = w >> 1, r = lane & 15, q = lane >> 4, sw = (r >> 1) & 7;
    const unsigned tag = __builtin_amdgcn_raw_buffer_load_b32(rws, EP_OFF, 0, SC1) + 1u;
    int pa[2], qa[2];
#pragma unroll
    for (int kk = 0; kk < 2; ++kk) {
        const int c = ((4 * kk + q) ^ sw) * 16;
        pa[kk] = lbase + (wp * 64 + r) * 128 + c;
        qa[kk] = lbase + HALF + (wq * 64 + r) * 128 + c;
    }
    asm volatile("" ::: ACLOB);
    acczero<0>();
    if (w == 0) lds_wr32(ctl + (lane & 1) * 4, (lane & 1) ? 0u : tag);
    const int off = wq * 8192 + lane * 16, moff = w * 4096 + lane * 16;
    Frags f;
    bar();
#pragma unroll 1
    for (int seg = 0; seg < 3; ++seg) {
        const int p0 = seg == 0 ? 0 : (seg == 1 ? 16 : 18), p1 = seg == 0 ? 16 : (seg == 1 ? 18 : NPOS);
        int pA[2], qA[2], pB[2], qB[2];
        { const int so = (p0 & 2) * SLOT;
          pA[0] = pa[0] + so; pA[1] = pa[1] + so; qA[0] = qa[0] + so; qA[1] = qa[1] + so;
          pB[0] = pA[0] ^ (2 * SLOT); pB[1] = pA[1] ^ (2 * SLOT); qB[0] = qA[0] ^ (2 * SLOT); qB[1] = qA[1] ^ (2 * SLOT); }
        rds<0>(f, pA, qA);
        int left = __builtin_amdgcn_readfirstlane(p1 - p0 - 2);
#pragma unroll 1
        for (;;) {
            lgkm_bar();
            mmrd<0, true>(f, pA, qA);
            if (__builtin_expect(left == 0, 0)) break;
            lgkm_bar();
            mmrd<0, false>(f, pB, qB);
            lgkm_bar();
            mmrd<0, true>(f, pB, qB);
            if (__builtin_expect(left == 2, 0)) break;
            if (__builtin_expect(left == NPOS - 20, 0)) {
                wait_vm<0>();
                if (lane == 0) __builtin_amdgcn_raw_buffer_store_b32(tag, rws, F2_OFF + (grp * 16 + m) * 16 + w * 4, 0, SC1);
            }
            left -= 4;
            lgkm_bar();
            mmrd<0, false>(f, pA, qA);
        }
        lgkm();
        if (seg == 2) break;
        if (seg == 0) {
            const unsigned sgn = im ? 0x80008000u : 0u;
            const int goff = grp * (512 * 1024) + (m * 2 + wp) * HALF;
            LAS unsigned char* lo = shm + wp * SLOT + off;
            tail1<0, 0>(f, [&](u32x4 v, int c) { v ^= sgn; __builtin_amdgcn_raw_buffer_store_b128(v, rws, off + c * 1024, goff, SC1); *(LAS u32x4*)(lo + c * 1024) = v; });
            acczero<0>();
            pa[0] = lbase + wp * 8192 + lane * 16; pa[1] = pa[0] + 1024;
            wait_vm<0>();
            wait_lgkm0();
        } else {
            mmcols<0, 4>(f);
#pragma unroll
            for (int i = 0; i < 4; ++i)
                __builtin_amdgcn_raw_buffer_store_b128((u32x4){~0u, ~0u, ~0u, ~0u}, rws, moff + i * 1024, mbox + (im ^ 1) * HALF, SC1);
        }
        bar();
        if (seg == 0 && w == 0 && lane < 32)
            __builtin_amdgcn_raw_buffer_store_b32(tag, rws, FL_OFF + (eb * 32 + lane) * 128 + (im * 16 + m) * 4, 0, SC1);
    }
    {
        const u32x4 lim = {0xfffffffeu, 0xfffffffeu, 0xfffffffeu, 0xfffffffeu};
        tail1<4, 4>(f, [&](u32x4 v, int c) { __builtin_amdgcn_raw_buffer_store_b128(__builtin_elementwise_min(v, lim), rws, moff + (c - 4) * 1024, mbox + (im ^ 1) * HALF, SC1); });
    }
    const unsigned one = (q == (r >> 2)) ? ((r & 1) ? 0x3f800000u : 0x00003f80u) : 0u;
    const u32x4 sel0 = {(r & 2) ? 0u : one, (r & 2) ? one : 0u, 0u, 0u}, sel1 = {0u, 0u, (r & 2) ? 0u : one, (r & 2) ? one : 0u};
    const __amdgpu_buffer_rsrc_t ry = mk_rsrc(y, S * D * 2);
    const int yoff = ((m * 128 + wq * 64 + im * 32 + r) * D + eb * 128 + wp * 64 + q * 8) * 2;
    u32x4 rcv[4];
    auto fetch = [&]() {
        asm volatile("" ::: "memory");
#pragma unroll
        for (int i = 0; i < 4; ++i) rcv[i] = __builtin_amdgcn_raw_buffer_load_b128(rws, moff + i * 1024, mbox + im * HALF, SC1);
        asm volatile("" ::: "memory");
    };
    {
        const int pB[2] = {pa[0] ^ (2 * SLOT), pa[1] ^ (2 * SLOT)}, qB[2] = {qa[0] ^ (2 * SLOT), qa[1] ^ (2 * SLOT)};
        asm volatile("" :: "v"(sel0), "v"(sel1), "v"(yoff), "v"(pB[0]), "v"(pB[1]), "v"(qB[0]), "v"(qB[1]));
        bar();      mmrdh<0, false>(f, pB, qB);
        lgkm_bar(); mmrdh<0, true>(f, pB, qB);
        lgkm_bar(); mmrdh<0, false>(f, pa, qa);
        lgkm_bar();
        fetch();
        mmrdh<0, true>(f, pa, qa);
        lgkm();
        mmcols<0, 2>(f);
    }
    auto complete = [&]() {
        const u32x4 mx = __builtin_elementwise_max(__builtin_elementwise_max(rcv[0], rcv[1]), __builtin_elementwise_max(rcv[2], rcv[3]));
        return __all(max(max(mx.x, mx.y), max(mx.z, mx.w)) != ~0u);
    };
    if (__builtin_expect(!complete(), 0)) {
        unsigned spins = 0;
#pragma unroll 1
        do fetch(); while (!complete() && ++spins < SPIN_MAX);
    }
    asm volatile("s_nop 1" : "+v"(rcv[0]), "+v"(rcv[1]), "+v"(rcv[2]), "+v"(rcv[3]));
    tail2<0>(rcv, sel0, sel1, [&](u32x4 v, int c) { __builtin_amdgcn_raw_buffer_store_b128(v, ry, yoff + (c >> 1) * 16 * D * 2 + (c & 1) * 64, 0, SC1); });
}

extern "C" void kernel_launch(void* const* d_in, const int* in_sizes, int n_in,
                              void* d_out, int out_size, void* d_ws, size_t ws_size,
                              hipStream_t stream) {
    fnet_fused<<<256, 512, 0, stream>>>((const bf16_t*)d_in[0], (const bf16_t*)d_in[1], (const bf16_t*)d_in[2],
                                        (const bf16_t*)d_in[3], (const bf16_t*)d_in[4], (unsigned char*)d_ws, (bf16_t*)d_out);
}
```

```cpp
#include <hip/hip_runtime.h>
#include <hip/hip_bf16.h>

typedef unsigned short bf16_t;
typedef short bf16x8 __attribute__((ext_vector_type(8)));
typedef unsigned u32x4 __attribute__((ext_vector_type(4)));
#define LAS __attribute__((address_space(3)))

constexpr int S = 2048, D = 1024;
constexpr int HALF = 16384, SLOT = 32768, NS = 4;
constexpr int CTL = NS * SLOT;
constexpr unsigned AB_BYTES = 2u * S * D * 2;
constexpr unsigned MB_OFF = AB_BYTES;
constexpr unsigned MB_BYTES = 128u * 32768;
constexpr unsigned FL_OFF = MB_OFF + MB_BYTES;
constexpr unsigned F2_OFF = FL_OFF + 32768;
constexpr unsigned EP_OFF = F2_OFF + 4096;
constexpr unsigned WS_USED = EP_OFF + 256;
constexpr unsigned SPIN_MAX = 1u << 18;
constexpr int SC1 = 16;
constexpr int RD_END = 24;
constexpr int NPOS = 46;
constexpr int ST2 = 29;

template <int N> __device__ __forceinline__ void wait_vm() { asm volatile("s_waitcnt vmcnt(%0)" :: "n"(N) : "memory"); }
__device__ __forceinline__ void wait_lgkm0() { asm volatile("" ::: "memory"); __builtin_amdgcn_s_waitcnt(0xc07f); asm volatile("" ::: "memory"); }
__device__ __forceinline__ void bar() { asm volatile("s_barrier" ::: "memory"); }
__device__ __forceinline__ void lgkm() { asm volatile("s_waitcnt lgkmcnt(0)" ::: "memory"); }
__device__ __forceinline__ void lgkm_bar() { asm volatile("s_waitcnt lgkmcnt(0)\n\ts_barrier" ::: "memory"); }
__device__ __forceinline__ __amdgpu_buffer_rsrc_t mk_rsrc(const void* p, unsigned bytes) {
    return __builtin_amdgcn_make_buffer_rsrc((void*)p, (short)0, (int)bytes, 0x00020000);
}
__device__ __forceinline__ int perm32(int r) { return (r & ~31) | ((r & 12) << 1) | ((r & 16) >> 2) | (r & 3); }
__device__ __forceinline__ unsigned lds_rd32(int addr) { unsigned v; asm volatile("ds_read_b32 %0, %1\n\ts_waitcnt lgkmcnt(0)" : "=v"(v) : "v"(addr) : "memory"); return v; }
__device__ __forceinline__ void lds_wr32(int addr, unsigned v) { asm volatile("ds_write_b32 %0, %1\n\ts_waitcnt lgkmcnt(0)" :: "v"(addr), "v"(v) : "memory"); }

#define ACLOB "a0","a1","a2","a3","a4","a5","a6","a7","a8","a9","a10","a11","a12","a13","a14","a15","a16","a17","a18","a19","a20","a21","a22","a23","a24","a25","a26","a27","a28","a29","a30","a31","a32","a33","a34","a35","a36","a37","a38","a39","a40","a41","a42","a43","a44","a45","a46","a47","a48","a49","a50","a51","a52","a53","a54","a55","a56","a57","a58","a59","a60","a61","a62","a63","a64","a65","a66","a67","a68","a69","a70","a71","a72","a73","a74","a75","a76","a77","a78","a79","a80","a81","a82","a83","a84","a85","a86","a87","a88","a89","a90","a91","a92","a93","a94","a95","a96","a97","a98","a99","a100","a101","a102","a103","a104","a105","a106","a107","a108","a109","a110","a111","a112","a113","a114","a115","a116","a117","a118","a119","a120","a121","a122","a123","a124","a125","a126","a127"
struct Frags { bf16x8 a[4][2], b[4][2]; };
constexpr int ACC(int i, int j) { return (i * 4 + j) * 4; }
constexpr int FA(int i, int kk) { return 64 + (i * 2 + kk) * 4; }
constexpr int FB(int j, int kk) { return 96 + (j * 2 + kk) * 4; }
template <int R> __device__ __forceinline__ void acczero() { if constexpr (R < 64) { asm volatile("v_accvgpr_write_b32 a[%c0], 0" :: "i"(R)); acczero<R + 1>(); } }
template <int R, bool TOA> __device__ __forceinline__ void rd(Frags& f, const int (&p)[2], const int (&q)[2]) {
    constexpr int kk = R & 1, t = R >> 1;
    constexpr bool isa = t == 0 || t > 4;
    constexpr int idx = t == 0 ? 0 : (t <= 4 ? t - 1 : t - 4);
    const int addr = isa ? p[kk] : q[kk];
    if constexpr (TOA) {
        constexpr int r = isa ? FA(idx, kk) : FB(idx, kk);
        asm volatile("ds_read_b128 a[%c1:%c2], %0 offset:%c3" :: "v"(addr), "i"(r), "i"(r + 3), "i"(idx * 2048 + SLOT));
    } else if constexpr (isa) asm volatile("ds_read_b128 %0, %1 offset:%c2" : "=v"(f.a[idx][kk]) : "v"(addr), "i"(idx * 2048));
    else asm volatile("ds_read_b128 %0, %1 offset:%c2" : "=v"(f.b[idx][kk]) : "v"(addr), "i"(idx * 2048));
}
template <int I, int J, int KK, bool FROMA> __device__ __forceinline__ void mm1(const Frags& f) {
    constexpr int c = ACC(I, J);
    if constexpr (FROMA) asm volatile("v_mfma_f32_16x16x32_bf16 a[%c0:%c1], a[%c2:%c3], a[%c4:%c5], a[%c0:%c1]" :: "i"(c), "i"(c + 3), "i"(FA(I, KK)), "i"(FA(I, KK) + 3), "i"(FB(J, KK)), "i"(FB(J, KK) + 3));
    else asm volatile("v_mfma_f32_16x16x32_bf16 a[%c2:%c3], %0, %1, a[%c2:%c3]" :: "v"(f.a[I][KK]), "v"(f.b[J][KK]), "i"(c), "i"(c + 3));
}
template <int R> __device__ __forceinline__ void rds(Frags& f, const int (&p)[2], const int (&q)[2]) {
    if constexpr (R < 16) { rd<R, false>(f, p, q); rds<R + 1>(f, p, q); }
}
template <int M, bool TOA> __device__ __forceinline__ void mmrd(Frags& f, const int (&p)[2], const int (&q)[2]) {
    if constexpr (M < 32) {
        mm1<(M >> 3), (((M >> 3) & 1) ? 3 - ((M >> 1) & 3) : ((M >> 1) & 3)), ((M ^ (M >> 1)) & 1), !TOA>(f);
        constexpr int r0 = (M * 16 + RD_END - 1) / RD_END, r1 = ((M + 1) * 16 + RD_END - 1) / RD_END;
        if constexpr (M < RD_END && r1 > r0) rd<r0, TOA>(f, p, q);
        mmrd<M + 1, TOA>(f, p, q);
    }
}
template <int M, bool TOA> __device__ __forceinline__ void mmrdh(Frags& f, const int (&p)[2], const int (&q)[2]) {
    if constexpr (M < 16) {
        mm1<(M >> 2), ((M >> 1) & 1), (M & 1), !TOA>(f);
        if constexpr (M < 12) rd<(M < 6 ? M : M + 4), TOA>(f, p, q);
        mmrdh<M + 1, TOA>(f, p, q);
    }
}
template <int J, int JEND> __device__ __forceinline__ void mmcols(const Frags& f) {
    if constexpr (J < JEND) {
        mm1<0, J, 0, true>(f); mm1<0, J, 1, true>(f); mm1<1, J, 0, true>(f); mm1<1, J, 1, true>(f);
        mm1<2, J, 0, true>(f); mm1<2, J, 1, true>(f); mm1<3, J, 0, true>(f); mm1<3, J, 1, true>(f);
        mmcols<J + 1, JEND>(f);
    }
}
template <int R> __device__ __forceinline__ unsigned accpk2() {
    unsigned d; float t;
    asm volatile("v_accvgpr_read_b32 %0, a[%c2]\n\tv_accvgpr_read_b32 %1, a[%c3]\n\tv_cvt_pk_bf16_f32 %0, %0, %1" : "=&v"(d), "=&v"(t) : "i"(R), "i"(R + 1));
    return d;
}
template <int C, int C0, class F> __device__ __forceinline__ void tail1(const Frags& f, F&& out) {
    if constexpr (C < 10) {
        constexpr int J = C >> 1, P = C & 1, O = C - 2, OJ = O >> 1, OP = O & 1;
        u32x4 v;
        if constexpr (C < 8) mm1<2 * P, J, 0, true>(f);
        if constexpr (O >= C0) v.x = accpk2<ACC(2 * OP, OJ)>();
        if constexpr (C < 8) mm1<2 * P, J, 1, true>(f);
        if constexpr (O >= C0) v.y = accpk2<ACC(2 * OP, OJ) + 2>();
        if constexpr (C < 8) mm1<2 * P + 1, J, 0, true>(f);
        if constexpr (O >= C0) v.z = accpk2<ACC(2 * OP + 1, OJ)>();
        if constexpr (C < 8) mm1<2 * P + 1, J, 1, true>(f);
        if constexpr (O >= C0) { v.w = accpk2<ACC(2 * OP + 1, OJ) + 2>(); out(v, O); asm volatile("" ::: "memory"); }
        if constexpr (C == C0 + 1) asm volatile("s_nop 3");
        tail1<C + 1, C0>(f, out);
    }
}
template <int C, class F> __device__ __forceinline__ void tail2(const u32x4 (&h)[4], const u32x4& sel0, const u32x4& sel1, F&& out) {
    if constexpr (C < 7) {
        constexpr int J = C >> 1, P = C & 1, O = C - 3, OJ = O >> 1, OP = O & 1;
        u32x4 v;
        if constexpr (C < 4) asm volatile("v_mfma_f32_16x16x32_bf16 a[%c2:%c3], %0, %1, a[%c2:%c3]" :: "v"(sel0), "v"(h[C & 3]), "i"(ACC(2 * P, J)), "i"(ACC(2 * P, J) + 3));
        if constexpr (C >= 3) { v.x = accpk2<ACC(2 * OP, OJ)>(); v.y = accpk2<ACC(2 * OP, OJ) + 2>(); }
        if constexpr (C < 4) asm volatile("v_mfma_f32_16x16x32_bf16 a[%c2:%c3], %0, %1, a[%c2:%c3]" :: "v"(sel1), "v"(h[C & 3]), "i"(ACC(2 * P + 1, J)), "i"(ACC(2 * P + 1, J) + 3));
        if constexpr (C >= 3) { v.z = accpk2<ACC(2 * OP + 1, OJ)>(); v.w = accpk2<ACC(2 * OP + 1, OJ) + 2>(); out(v, O); asm volatile("" ::: "memory"); }
        if constexpr (C == 2) asm volatile("s_nop 3");
        tail2<C + 1>(h, sel0, sel1, out);
    }
}

template <int AUX>
__device__ __forceinline__ void issue4(__amdgpu_buffer_rsrc_t rs, LAS unsigned char* dst, const int (&v)[4], int soff) {
    __builtin_amdgcn_raw_ptr_buffer_load_lds(rs, dst, 16, v[0], soff, 0, AUX);
    __builtin_amdgcn_raw_ptr_buffer_load_lds(rs, dst, 16, v[1], soff, 1024, AUX);
    __builtin_amdgcn_raw_ptr_buffer_load_lds(rs, dst, 16, v[2], soff, 2048, AUX);
    __builtin_amdgcn_raw_ptr_buffer_load_lds(rs, dst, 16, v[3], soff, 3072, AUX);
}

__device__ __forceinline__ int lf_order(int p, int c, int n) {
    const int g = (p - 1) >> 2, o = (p - 1) & 3;
    const bool last = p >= n - 3;
    const int i = last ? o : o - (o > c);
    int v = g * 4 + i + (i >= c);
    if (!last && o == c) v = (g + 1) * 4 + c;
    return p == 0 ? c : v;
}

extern "C" __global__ __launch_bounds__(512, 2) void fnet_fused(const bf16_t* __restrict__ x, const bf16_t* __restrict__ sre, const bf16_t* __restrict__ sim,
                                                               const bf16_t* __restrict__ wre, const bf16_t* __restrict__ wim,
                                                               unsigned char* __restrict__ ws, bf16_t* __restrict__ y) {
    __shared__ __attribute__((aligned(16))) unsigned char shm_[CTL + 1280];
    LAS unsigned char* shm = (LAS unsigned char*)shm_;
    const int lbase = (int)(unsigned)(size_t)shm, ctl = lbase + CTL;
    const int tid = threadIdx.x, lane = tid & 63;
    const int w = __builtin_amdgcn_readfirstlane(tid >> 6);
    const int b = blockIdx.x, xcd = b & 7, rr = b >> 3;
    const int xe = xcd & 1, xt = (xcd >> 1) & 1, im = xcd >> 2;
    const int eb = xe * 4 + (rr & 3), m = xt * 8 + (rr >> 2), cls = (eb + m) & 3, phi = xe ^ xt;
    const int grp = eb * 2 + im;
    const int mbox = MB_OFF + (eb * 16 + m) * 32768;
    const __amdgpu_buffer_rsrc_t rws = mk_rsrc(ws, WS_USED);

    if (__builtin_expect(w >= 4, 1)) {
        const int lw = w - 4;
        LAS unsigned char* dst0 = shm + lw * 4096;
        __amdgpu_buffer_rsrc_t rp = mk_rsrc(x, S * D * 2);
        __amdgpu_buffer_rsrc_t rq = mk_rsrc(im ? wim : wre, D * D * 2);
        int vp[4], vq[4];
#pragma unroll
        for (int i = 0; i < 4; ++i) {
            const int rho = lw * 32 + i * 8 + (lane >> 3);
            vp[i] = vq[i] = perm32(rho) * (D * 2) + ((lane & 7) ^ ((rho >> 1) & 7)) * 16 - i * 1024;
        }
        {
            const int k0 = (cls ^ (phi << 2)) * 128;
            issue4<SC1>(rp, dst0, vp, m * 128 * D * 2 + k0);
            issue4<0>(rq, dst0 + HALF, vq, eb * 128 * D * 2 + k0);
        }
        int tabp, tabq;
        {
            const int v = lf_order(lane & 31, cls, 32) ^ (phi << 2);
            const int u = v >> 1, blk = (u >> 1) + 8 * (u & 1), kk = 2 * blk + (v & 1);
            const int sk = m == 15 ? 7 : 15;
            const bool keep = lane < 32 && blk != m && blk != sk;
            const unsigned long long bal = __ballot(keep);
            const int posn = keep ? 18 + __builtin_amdgcn_mbcnt_hi((unsigned)(bal >> 32), __builtin_amdgcn_mbcnt_lo((unsigned)bal, 0)) : 63;
            int k = __builtin_amdgcn_ds_permute(posn * 4, kk);
            if (lane < 18) k = 2 * m + (lane & 1);
            if (lane >= NPOS) k = 2 * sk + (lane & 1);
            tabp = (lane >= NPOS + 2 ? grp ^ 1 : grp) * (512 * 1024) + k * HALF;
            tabq = m * 128 * S * 2 + k * 128;
            if (lane < 16) { const int k1 = (lf_order(lane, cls, 16) ^ (phi << 2)) * 128; tabp = m * 128 * D * 2 + k1; tabq = eb * 128 * D * 2 + k1; }
        }
        auto issP = [&](int pos) { issue4<SC1>(rp, dst0 + (pos & (NS - 1)) * SLOT, vp, __builtin_amdgcn_readlane(tabp, pos)); };
        auto issQ = [&](int pos) { issue4<0>(rq, dst0 + (pos & (NS - 1)) * SLOT + HALF, vq, __builtin_amdgcn_readlane(tabq, pos)); };
        unsigned want = 0;
        auto poll = [&](const unsigned char* fp, bool need) {
#pragma unroll 1
            for (unsigned spins = 0; spins < SPIN_MAX; ++spins) {
                unsigned f;
                asm volatile("global_load_dword %0, %1, off sc1\n\ts_waitcnt vmcnt(0)" : "=&v"(f) : "v"(fp) : "memory");
                if (__all(!need || f == want)) break;
                __builtin_amdgcn_s_sleep(1);
            }
        };
        const unsigned char* fp = ws + FL_OFF + (grp * 16 + m) * 128 + (lane & 31) * 4;
        issP(1); issQ(1);
        wait_vm<8>(); bar();
        issP(2); issQ(2);
        int pos = 3;
        auto run = [&](int to) {
#pragma unroll 1
            for (; pos < to; ++pos) {
                issP(pos); issQ(pos);
                wait_vm<16>(); bar();
            }
        };
#pragma unroll 1
        for (; pos < 8; ++pos) {
            issP(pos); issQ(pos);
            __builtin_amdgcn_s_sleep(3);
            wait_vm<16>(); bar();
        }
        const __amdgpu_buffer_rsrc_t rs1 = mk_rsrc(im ? sim : sre, S * S * 2), rs2 = mk_rsrc(im ? sre : sim, S * S * 2);
        run(16);
        rp = rws; rq = rs1;
#pragma unroll
        for (int i = 0; i < 4; ++i) {
            const int rho = lw * 32 + i * 8 + (lane >> 3);
            vq[i] = (rho ^ (im << 5)) * (S * 2) + ((lane & 7) ^ ((rho >> 1) & 7)) * 16 - i * 1024;
            vp[i] = lw * 4096 + lane * 16;
        }
        issQ(16);
        if (lw < 2) {
            __builtin_amdgcn_raw_ptr_buffer_load_lds(rq, shm + CTL + 1024, 4, (lw * 64 + lane) * (S * 2), __builtin_amdgcn_readlane(tabq, 18), 0, 0);
            wait_vm<13>(); bar();
            issQ(17); wait_vm<9>(); bar();
            wait_vm<5>();
        } else {
            wait_vm<12>(); bar();
            issQ(17); wait_vm<8>(); bar();
            wait_vm<4>();
        }
        want = lds_rd32(ctl);
        bar();
        issQ(18); issQ(19); wait_vm<8>();
        bar();
        if (lw == 0) {
            poll(fp, ((lane >> 4) & 1) == im && (lane & 4) == 0);
            lds_wr32(ctl + 4, 1u);
        } else {
#pragma unroll 1
            for (unsigned spins = 0; spins < (SPIN_MAX << 4); ++spins) {
                if (__builtin_amdgcn_readfirstlane(lds_rd32(ctl + 4)) == 1u) break;
                __builtin_amdgcn_s_sleep(1);
            }
        }
        issP(18); issP(19); wait_vm<4>();
        bar();
        issP(20); issQ(20);
        pos = 21;
        run(ST2 - 2);
        if (lw == 0) __builtin_amdgcn_raw_ptr_buffer_load_lds(rws, shm + CTL + 256, 4, (lane & 31) * 4, FL_OFF + (grp * 16 + m) * 128, 0, SC1);
        run(ST2);
        if (!__all(lds_rd32(ctl + 256 + lane * 4) == want)) poll(fp, true);
        if (lw == 1) {
            const int row = ((eb & 2) << 5) + (im << 5) + (eb & 1) * 16 + ((lane >> 1) & 15), vo = row * (S * 2) + (lane & 1) * 128;
            if (lane < 32) __builtin_amdgcn_raw_ptr_buffer_load_lds(rs1, shm + CTL + 1024, 4, vo, __builtin_amdgcn_readlane(tabq, NPOS), 0, 0);
            else __builtin_amdgcn_raw_ptr_buffer_load_lds(rs2, shm + CTL + 1024, 4, vo, __builtin_amdgcn_readlane(tabq, NPOS), 0, 0);
        }
        run(NPOS);
        if (lw == 0) __builtin_amdgcn_raw_ptr_buffer_load_lds(rws, shm + CTL + 768, 4, (lane & 3) * 4, F2_OFF + ((grp ^ 1) * 16 + m) * 16, 0, SC1);
        int vh[2];
#pragma unroll
        for (int i = 0; i < 2; ++i) {
            const int rho = (lw >> 1) * 64 + (lw & 1) * 16 + i * 8 + (lane >> 3);
            vh[i] = (rho ^ (im << 5)) * (S * 2) + ((lane & 7) ^ ((rho >> 1) & 7)) * 16 - i * 1024;
        }
        auto issH = [&](int p, __amdgpu_buffer_rsrc_t r) {
            issP(p);
            LAS unsigned char* dst = shm + (p & (NS - 1)) * SLOT + HALF + (lw >> 1) * 8192 + (lw & 1) * 2048;
            const int so = __builtin_amdgcn_readlane(tabq, p);
            __builtin_amdgcn_raw_ptr_buffer_load_lds(r, dst, 16, vh[0], so, 0, 0);
            __builtin_amdgcn_raw_ptr_buffer_load_lds(r, dst, 16, vh[1], so, 1024, 0);
        };
        issH(46, rs1); wait_vm<14>(); bar();
        issH(47, rs1); wait_vm<12>(); bar();
        issH(48, rs2); wait_vm<12>(); bar();
        issH(49, rs2); wait_vm<12>(); bar();
        if (!__all(lds_rd32(ctl + 768 + lane * 4) == want)) poll(ws + F2_OFF + ((grp ^ 1) * 16 + m) * 16 + (lane & 3) * 4, true);
        wait_vm<6>(); bar();
        wait_vm<0>(); bar();
        if (b == 0 && lw == 0) {
#pragma unroll 1
            for (unsigned spins = 0; spins < SPIN_MAX; ++spins) {
                asm volatile("" ::: "memory");
                const u32x4 f = __builtin_amdgcn_raw_buffer_load_b128(rws, FL_OFF + (lane >> 3) * 4096 + (lane & 7) * 16, 0, SC1);
                if (__all(f.x == want && f.y == want && f.z == want && f.w == want)) break;
                __builtin_amdgcn_s_sleep(2);
            }
            if (lane == 0) __builtin_amdgcn_raw_buffer_store_b32(want, rws, EP_OFF, 0, SC1);
        }
        return;
    }

    const int wp = w & 1, wq = w >> 1, r = lane & 15, q = lane >> 4, sw = (r >> 1) & 7;
    const unsigned tag = __builtin_amdgcn_raw_buffer_load_b32(rws, EP_OFF, 0, SC1) * 1664525u + 1013904223u;
    int pa[2], qa[2];
#pragma unroll
    for (int kk = 0; kk < 2; ++kk) {
        const int c = ((4 * kk + q) ^ sw) * 16;
        pa[kk] = lbase + (wp * 64 + r) * 128 + c;
        qa[kk] = lbase + HALF + (wq * 64 + r) * 128 + c;
    }
    asm volatile("" ::: ACLOB);
    acczero<0>();
    if (w == 0) lds_wr32(ctl + (lane & 1) * 4, (lane & 1) ? 0u : tag);
    const int off = wq * 8192 + lane * 16, moff = w * 4096 + lane * 16;
    Frags f;
    bar();
#pragma unroll 1
    for (int seg = 0; seg < 3; ++seg) {
        const int p0 = seg == 0 ? 0 : (seg == 1 ? 16 : 18), p1 = seg == 0 ? 16 : (seg == 1 ? 18 : NPOS);
        int pA[2], qA[2], pB[2], qB[2];
        { const int so = (p0 & 2) * SLOT;
          pA[0] = pa[0] + so; pA[1] = pa[1] + so; qA[0] = qa[0] + so; qA[1] = qa[1] + so;
          pB[0] = pA[0] ^ (2 * SLOT); pB[1] = pA[1] ^ (2 * SLOT); qB[0] = qA[0] ^ (2 * SLOT); qB[1] = qA[1] ^ (2 * SLOT); }
        rds<0>(f, pA, qA);
        int left = __builtin_amdgcn_readfirstlane(p1 - p0 - 2);
#pragma unroll 1
        for (;;) {
            lgkm_bar();
            mmrd<0, true>(f, pA, qA);
            if (__builtin_expect(left == 0, 0)) break;
            lgkm_bar();
            mmrd<0, false>(f, pB, qB);
            lgkm_bar();
            mmrd<0, true>(f, pB, qB);
            if (__builtin_expect(left == 2, 0)) break;
            if (__builtin_expect(left == NPOS - 20, 0)) {
                wait_vm<0>();
                if (lane == 0) __builtin_amdgcn_raw_buffer_store_b32(tag, rws, F2_OFF + (grp * 16 + m) * 16 + w * 4, 0, SC1);
            }
            left -= 4;
            lgkm_bar();
            mmrd<0, false>(f, pA, qA);
        }
        lgkm();
        if (seg == 2) break;
        if (seg == 0) {
            const unsigned sgn = im ? 0x80008000u : 0u;
            const int goff = grp * (512 * 1024) + (m * 2 + wp) * HALF;
            LAS unsigned char* lo = shm + wp * SLOT + off;
            tail1<0, 0>(f, [&](u32x4 v, int c) { v ^= sgn; __builtin_amdgcn_raw_buffer_store_b128(v, rws, off + c * 1024, goff, SC1); *(LAS u32x4*)(lo + c * 1024) = v; });
            acczero<0>();
            pa[0] = lbase + wp * 8192 + lane * 16; pa[1] = pa[0] + 1024;
            wait_vm<0>();
            wait_lgkm0();
        } else {
            mmcols<0, 4>(f);
#pragma unroll
            for (int i = 0; i < 4; ++i)
                __builtin_amdgcn_raw_buffer_store_b128((u32x4){~0u, ~0u, ~0u, ~0u}, rws, moff + i * 1024, mbox + (im ^ 1) * HALF, SC1);
        }
        bar();
        if (seg == 0 && w == 0 && lane < 32)
            __builtin_amdgcn_raw_buffer_store_b32(tag, rws, FL_OFF + (eb * 32 + lane) * 128 + (im * 16 + m) * 4, 0, SC1);
    }
    {
        const u32x4 lim = {0xfffffffeu, 0xfffffffeu, 0xfffffffeu, 0xfffffffeu};
        tail1<4, 4>(f, [&](u32x4 v, int c) { __builtin_amdgcn_raw_buffer_store_b128(__builtin_elementwise_min(v, lim), rws, moff + (c - 4) * 1024, mbox + (im ^ 1) * HALF, SC1); });
    }
    const unsigned one = (q == (r >> 2)) ? ((r & 1) ? 0x3f800000u : 0x00003f80u) : 0u;
    const u32x4 sel0 = {(r & 2) ? 0u : one, (r & 2) ? one : 0u, 0u, 0u}, sel1 = {0u, 0u, (r & 2) ? 0u : one, (r & 2) ? one : 0u};
    const __amdgpu_buffer_rsrc_t ry = mk_rsrc(y, S * D * 2);
    const int yoff = ((m * 128 + wq * 64 + im * 32 + r) * D + eb * 128 + wp * 64 + q * 8) * 2;
    u32x4 rcv[4];
    auto fetch = [&]() {
        asm volatile("" ::: "memory");
#pragma unroll
        for (int i = 0; i < 4; ++i) rcv[i] = __builtin_amdgcn_raw_buffer_load_b128(rws, moff + i * 1024, mbox + im * HALF, SC1);
        asm volatile("" ::: "memory");
    };
    {
        const int pB[2] = {pa[0] ^ (2 * SLOT), pa[1] ^ (2 * SLOT)}, qB[2] = {qa[0] ^ (2 * SLOT), qa[1] ^ (2 * SLOT)};
        asm volatile("" :: "v"(sel0), "v"(sel1), "v"(yoff), "v"(pB[0]), "v"(pB[1]), "v"(qB[0]), "v"(qB[1]));
        bar();      mmrdh<0, false>(f, pB, qB);
        lgkm_bar(); mmrdh<0, true>(f, pB, qB);
        lgkm_bar(); mmrdh<0, false>(f, pa, qa);
        lgkm_bar();
        fetch();
        mmrdh<0, true>(f, pa, qa);
        lgkm();
        mmcols<0, 2>(f);
    }
    auto complete = [&]() {
        const u32x4 mx = __builtin_elementwise_max(__builtin_elementwise_max(rcv[0], rcv[1]), __builtin_elementwise_max(rcv[2], rcv[3]));
        return __all(max(max(mx.x, mx.y), max(mx.z, mx.w)) != ~0u);
    };
    if (__builtin_expect(!complete(), 0)) {
        unsigned spins = 0;
#pragma unroll 1
        do fetch(); while (!complete() && ++spins < SPIN_MAX);
    }
    asm volatile("s_nop 1" : "+v"(rcv[0]), "+v"(rcv[1]), "+v"(rcv[2]), "+v"(rcv[3]));
    tail2<0>(rcv, sel0, sel1, [&](u32x4 v, int c) { __builtin_amdgcn_raw_buffer_store_b128(v, ry, yoff + (c >> 1) * 16 * D * 2 + (c & 1) * 64, 0, SC1); });
}

extern "C" void kernel_launch(void* const* d_in, const int* in_sizes, int n_in,
                              void* d_out, int out_size, void* d_ws, size_t ws_size,
                              hipStream_t stream) {
    fnet_fused<<<256, 512, 0, stream>>>((const bf16_t*)d_in[0], (const bf16_t*)d_in[1], (const bf16_t*)d_in[2],
                                        (const bf16_t*)d_in[3], (const bf16_t*)d_in[4], (unsigned char*)d_ws, (bf16_t*)d_out);
}
```
